```python
import torch

def _softmax(x, axis=-1):
    return torch.softmax(x, dim=axis)
def _gelu(x):
    return torch.nn.functional.gelu(x, approximate="tanh")
def _silu(x):
    return torch.nn.functional.silu(x)
def _sigmoid(x):
    return torch.sigmoid(x)
def _layernorm(x, axis=-1, eps=1e-5):
    m = x.mean(dim=axis, keepdim=True)
    v = x.var(dim=axis, keepdim=True, unbiased=False)
    return (x - m) / torch.sqrt(v + eps)
def _scatter_add(tgt, idx, src, axis=0):
    return tgt.clone().index_add_(axis, idx, src)
def _segment_sum(src, seg, n):
    out = torch.zeros((n,) + src.shape[1:], dtype=src.dtype)
    return out.index_add_(0, seg, src)
def _pad(x, pw):
    flat = tuple(v for pair in reversed(pw) for v in pair)
    return torch.nn.functional.pad(x, flat)

kernel_name = 'recurrent_scan__mha_block_cached__dense_activation_gelu__decode'

def setup_inputs():

    _w = (lambda a: (lambda t: t.to(torch.bfloat16) if t.is_floating_point() else t)(torch.as_tensor(a)))
    def _A_setup():
        B, Sq, D = 8, 1, 640
        H, Sk, Dh = 32, 4096, 128
        return {
            "x": _w((lambda *s: torch.randn(*s, dtype=torch.bfloat16) * 0.1)(B, Sq, D)),
            "k_cache": _w((lambda *s: torch.randn(*s, dtype=torch.bfloat16) * 0.8)(B, H, Sk, Dh)),
            "v_cache": _w((lambda *s: torch.randn(*s, dtype=torch.bfloat16) * 3.2)(B, H, Sk, Dh)),
            "wq": _w((lambda *s: torch.randn(*s, dtype=torch.bfloat16) * 0.8)(D, H * Dh) * (D ** -0.5)),
            "wo": _w((lambda *s: torch.randn(*s, dtype=torch.bfloat16) * 3.2)(H * Dh, D) * ((H * Dh) ** -0.5)),
        }
    _A_in = _A_setup()

    def _B_setup():
        B, S, D, F = 8, 1, 640, 1728
        return {
            "x": _w((lambda *s: torch.randn(*s, dtype=torch.bfloat16) * 0.1)(B, S, D)),
            "w1": _w((lambda *s: torch.randn(*s, dtype=torch.bfloat16) * 3.2)(D, F) * (D ** -0.5)),
            "b1": _w((lambda *s: torch.randn(*s, dtype=torch.bfloat16) * 3.2)(F) * 0.01),
            "w2": _w((lambda *s: torch.randn(*s, dtype=torch.bfloat16) * 3.2)(F, D) * (F ** -0.5)),
            "b2": _w((lambda *s: torch.randn(*s, dtype=torch.bfloat16) * 3.2)(D) * 0.01),
        }
    _B_in = _B_setup()

    return {
        "A_x": _A_in["x"],
        "A_k_cache": _A_in["k_cache"],
        "A_v_cache": _A_in["v_cache"],
        "A_wq": _A_in["wq"],
        "A_wo": _A_in["wo"],
        "B_x": _B_in["x"],
        "B_w1": _B_in["w1"],
        "B_b1": _B_in["b1"],
        "B_w2": _B_in["w2"],
        "B_b2": _B_in["b2"],
    }

def reference(A_x, A_k_cache, A_v_cache, A_wq, A_wo, B_x, B_w1, B_b1, B_w2, B_b2):
    def _A_ref(x, k_cache, v_cache, wq, wo):
        B, Sq, D = x.shape
        H, Dh = 32, 128
        q = (lambda x, *d: x.permute(*d))((x @ wq).reshape(B, Sq, H, Dh), 0, 2, 1, 3)
        scores = torch.einsum("bhqd,bhkd->bhqk", q, k_cache) * (Dh ** -0.5)
        p = _softmax(scores, axis=-1)
        o = torch.einsum("bhqk,bhkd->bhqd", p, v_cache)
        return (lambda x, *d: x.permute(*d))(o, 0, 2, 1, 3).reshape(B, Sq, H * Dh) @ wo

    def _B_ref(x, w1, b1, w2, b2):
        h = torch.einsum("bsd,df->bsf", x, w1) + b1
        h = _gelu(h)
        return torch.einsum("bsf,fd->bsd", h, w2) + b2

    x = A_x
    xn = _layernorm(x, axis=-1)
    s = _A_ref(xn, A_k_cache, A_v_cache, A_wq, A_wo)
    g = _silu(_B_ref(xn, B_w1, B_b1, B_w2, B_b2))
    return x + s * g
```

```cpp
#include <hip/hip_runtime.h>
#include <hip/hip_bf16.h>
#include <stdint.h>

#define KV_NT 1

namespace {

constexpr int NB = 8, DM = 640, NH = 32, SK = 4096, DH = 128, FF = 1728;
constexpr int NW = 16;
constexpr int NT = NW * 64;
constexpr int FS = FF / NH;
constexpr int ROWS_W = DM / NW;
constexpr int NCHUNK = SK / 16 / NW;

typedef unsigned u32x4 __attribute__((ext_vector_type(4)));
typedef float f32x4 __attribute__((ext_vector_type(4)));
typedef __bf16 bf16x2 __attribute__((ext_vector_type(2)));

__device__ __forceinline__ float bf_lo(unsigned u) { return __builtin_bit_cast(float, u << 16); }
__device__ __forceinline__ float bf_hi(unsigned u) { return __builtin_bit_cast(float, u & 0xffff0000u); }
__device__ __forceinline__ float bf1(unsigned short u) { return __builtin_bit_cast(float, (unsigned)u << 16); }
__device__ __forceinline__ float rbf(float x) { return (float)(__bf16)x; }
__device__ __forceinline__ unsigned short tobf(float x) { return __builtin_bit_cast(unsigned short, (__bf16)x); }
__device__ __forceinline__ unsigned pack2(float lo, float hi) { return (unsigned)tobf(lo) | ((unsigned)tobf(hi) << 16); }

__device__ __forceinline__ float dot2(unsigned k, unsigned q, float acc) {
  return __builtin_amdgcn_fdot2_f32_bf16(__builtin_bit_cast(bf16x2, k), __builtin_bit_cast(bf16x2, q), acc, false);
}
template <int CTRL> __device__ __forceinline__ float dpp(float x) {
  return __builtin_bit_cast(float, __builtin_amdgcn_mov_dpp(__builtin_bit_cast(int, x), CTRL, 0xf, 0xf, true));
}
__device__ __forceinline__ float row16_sum(float x) {
  x += dpp<0xB1>(x);
  x += dpp<0x4E>(x);
  x += dpp<0x141>(x);
  x += dpp<0x140>(x);
  return x;
}
__device__ __forceinline__ float xrow_sum(float x) {
  auto s = __builtin_amdgcn_permlane16_swap(__float_as_uint(x), __float_as_uint(x), false, false);
  x = __uint_as_float(s[0]) + __uint_as_float(s[1]);
  auto t = __builtin_amdgcn_permlane32_swap(__float_as_uint(x), __float_as_uint(x), false, false);
  return __uint_as_float(t[0]) + __uint_as_float(t[1]);
}
__device__ __forceinline__ float xrow_max(float x) {
  auto s = __builtin_amdgcn_permlane16_swap(__float_as_uint(x), __float_as_uint(x), false, false);
  x = fmaxf(__uint_as_float(s[0]), __uint_as_float(s[1]));
  auto t = __builtin_amdgcn_permlane32_swap(__float_as_uint(x), __float_as_uint(x), false, false);
  return fmaxf(__uint_as_float(t[0]), __uint_as_float(t[1]));
}
__device__ __forceinline__ float wave_sum(float x) { return xrow_sum(row16_sum(x)); }

__device__ __forceinline__ u32x4 ld_kv(const u32x4* p) {
  return __builtin_nontemporal_load(p);
}

constexpr int L_XN = 0;
constexpr int L_QP = L_XN + DM;
constexpr int L_HP = L_QP + NW * DH;
constexpr int L_HA = L_HP + NW * 64;
constexpr int L_MG = L_HA + 64;
constexpr int MG_REC = DH + 4;
constexpr int L_O = L_MG + NW * MG_REC;
constexpr int L_SP = L_O + DH;
constexpr int L_MP = L_SP + NW * DM;
constexpr int L_END = L_MP + NW * DM;

struct KV4 { u32x4 k[4]; u32x4 v[4]; };

__global__ void __launch_bounds__(NT, 4) fused_attn_kernel(
    const unsigned short* __restrict__ x, const unsigned short* __restrict__ kc, const unsigned short* __restrict__ vc,
    const unsigned short* __restrict__ wq, const unsigned short* __restrict__ wo, const unsigned short* __restrict__ w1,
    const unsigned short* __restrict__ b1, const unsigned short* __restrict__ w2, float* __restrict__ spart,
    float* __restrict__ mpart) {
  __shared__ __attribute__((aligned(16))) float lds[L_END];
  const int tid = threadIdx.x, lane = tid & 63;
  const int w = __builtin_amdgcn_readfirstlane(tid >> 6);
  const int r = lane >> 4, c = lane & 15;
  const int bid = blockIdx.x;
  const int h = (bid & 7) * 4 + ((bid >> 3) & 3);
  const int b = bid >> 5;

  const unsigned short* xb = x + b * DM;
  const u32x4 xa = *reinterpret_cast<const u32x4*>(xb + lane * 8);
  const unsigned xt = *reinterpret_cast<const unsigned*>(xb + 512 + lane * 2);

  u32x4 wqr[10];
  {
    const unsigned short* p = wq + (size_t)(ROWS_W * w + r) * (NH * DH) + h * DH + c * 8;
#pragma unroll
    for (int i = 0; i < 10; ++i) wqr[i] = *reinterpret_cast<const u32x4*>(p + (size_t)(4 * i) * (NH * DH));
  }
  u32x4 w1r[5];
  const int r8 = lane >> 3, c8 = lane & 7;
  const int chunk0 = (FS * h) >> 3;
  {
    const int ch = min(chunk0 + c8, FF / 8 - 1);
    const unsigned short* p = w1 + (size_t)(ROWS_W * w + r8) * FF + ch * 8;
#pragma unroll
    for (int i = 0; i < 5; ++i) w1r[i] = *reinterpret_cast<const u32x4*>(p + (size_t)(8 * i) * FF);
  }
  const size_t kvbase = ((size_t)(b * NH + h) * SK + (size_t)16 * w + r) * DH + c * 8;
  const u32x4* kp = reinterpret_cast<const u32x4*>(kc + kvbase);
  const u32x4* vp = reinterpret_cast<const u32x4*>(vc + kvbase);
  constexpr int CH_STRIDE = NW * 16 * DH / 8;
  constexpr int T_STRIDE = 4 * DH / 8;
  KV4 A, Bf;
#pragma unroll
  for (int t = 0; t < 4; ++t) A.k[t] = ld_kv(kp + t * T_STRIDE);
#pragma unroll
  for (int t = 0; t < 4; ++t) A.v[t] = ld_kv(vp + t * T_STRIDE);
  __builtin_amdgcn_sched_barrier(0);

  {
    float xv[10];
#pragma unroll
    for (int k = 0; k < 4; ++k) { xv[2 * k] = bf_lo(xa[k]); xv[2 * k + 1] = bf_hi(xa[k]); }
    xv[8] = bf_lo(xt); xv[9] = bf_hi(xt);
    float s = 0.f;
#pragma unroll
    for (int k = 0; k < 10; ++k) s += xv[k];
    const float mean = wave_sum(s) * (1.0f / DM);
    float ss = 0.f;
#pragma unroll
    for (int k = 0; k < 10; ++k) { const float d = xv[k] - mean; ss = fmaf(d, d, ss); }
    const float var = wave_sum(ss) * (1.0f / DM);
    const float mb = rbf(mean);
    const float sd = rbf(sqrtf(rbf(rbf(var) + 1e-5f)));
    float xn[10];
#pragma unroll
    for (int k = 0; k < 10; ++k) xn[k] = rbf(rbf(xv[k] - mb) / sd);
    *reinterpret_cast<f32x4*>(&lds[L_XN + lane * 8]) = f32x4{xn[0], xn[1], xn[2], xn[3]};
    *reinterpret_cast<f32x4*>(&lds[L_XN + lane * 8 + 4]) = f32x4{xn[4], xn[5], xn[6], xn[7]};
    lds[L_XN + 512 + lane * 2] = xn[8];
    lds[L_XN + 512 + lane * 2 + 1] = xn[9];
  }
  __builtin_amdgcn_fence(__ATOMIC_RELEASE, "wavefront");
  asm volatile("s_waitcnt lgkmcnt(0)" ::: "memory");

  {
    float acc[8];
#pragma unroll
    for (int k = 0; k < 8; ++k) acc[k] = 0.f;
#pragma unroll
    for (int i = 0; i < 10; ++i) {
      const float xv = lds[L_XN + ROWS_W * w + 4 * i + r];
#pragma unroll
      for (int k = 0; k < 4; ++k) {
        acc[2 * k] = fmaf(xv, bf_lo(wqr[i][k]), acc[2 * k]);
        acc[2 * k + 1] = fmaf(xv, bf_hi(wqr[i][k]), acc[2 * k + 1]);
      }
    }
#pragma unroll
    for (int k = 0; k < 8; ++k) acc[k] = xrow_sum(acc[k]);
    if (r == 0) {
      *reinterpret_cast<f32x4*>(&lds[L_QP + w * DH + c * 8]) = f32x4{acc[0], acc[1], acc[2], acc[3]};
      *reinterpret_cast<f32x4*>(&lds[L_QP + w * DH + c * 8 + 4]) = f32x4{acc[4], acc[5], acc[6], acc[7]};
    }
  }
#pragma unroll
  for (int t = 0; t < 4; ++t) Bf.k[t] = ld_kv(kp + CH_STRIDE + t * T_STRIDE);
#pragma unroll
  for (int t = 0; t < 4; ++t) Bf.v[t] = ld_kv(vp + CH_STRIDE + t * T_STRIDE);
  __builtin_amdgcn_sched_barrier(0);
  {
    float acc[8];
#pragma unroll
    for (int k = 0; k < 8; ++k) acc[k] = 0.f;
#pragma unroll
    for (int i = 0; i < 5; ++i) {
      const float xv = lds[L_XN + ROWS_W * w + 8 * i + r8];
#pragma unroll
      for (int k = 0; k < 4; ++k) {
        acc[2 * k] = fmaf(xv, bf_lo(w1r[i][k]), acc[2 * k]);
        acc[2 * k + 1] = fmaf(xv, bf_hi(w1r[i][k]), acc[2 * k + 1]);
      }
    }
#pragma unroll
    for (int k = 0; k < 8; ++k) {
      float v = acc[k];
      v += dpp<0x128>(v);
      acc[k] = xrow_sum(v);
    }
    if (lane < 8) {
      *reinterpret_cast<f32x4*>(&lds[L_HP + w * 64 + c8 * 8]) = f32x4{acc[0], acc[1], acc[2], acc[3]};
      *reinterpret_cast<f32x4*>(&lds[L_HP + w * 64 + c8 * 8 + 4]) = f32x4{acc[4], acc[5], acc[6], acc[7]};
    }
  }
  __syncthreads();
  u32x4 q;
  {
    f32x4 s0 = {0.f, 0.f, 0.f, 0.f}, s1 = {0.f, 0.f, 0.f, 0.f};
#pragma unroll
    for (int ww = 0; ww < NW / 4; ++ww) {
      s0 += *reinterpret_cast<const f32x4*>(&lds[L_QP + (r * (NW / 4) + ww) * DH + c * 8]);
      s1 += *reinterpret_cast<const f32x4*>(&lds[L_QP + (r * (NW / 4) + ww) * DH + c * 8 + 4]);
    }
#pragma unroll
    for (int k = 0; k < 4; ++k) { s0[k] = xrow_sum(s0[k]); s1[k] = xrow_sum(s1[k]); }
    q[0] = pack2(s0[0], s0[1]); q[1] = pack2(s0[2], s0[3]); q[2] = pack2(s1[0], s1[1]); q[3] = pack2(s1[2], s1[3]);
  }
  asm volatile("" : "+v"(q));
  if (w == 0) {
    const int off = FS * h - chunk0 * 8;
    float hv = 0.f;
    if (lane < FS) {
      float s = 0.f;
#pragma unroll
      for (int ww = 0; ww < NW; ++ww) s += lds[L_HP + ww * 64 + off + lane];
      const float hb = rbf(rbf(s) + bf1(b1[FS * h + lane]));
      const float u = 0.7978845608028654f * (hb + 0.044715f * hb * hb * hb);
      hv = rbf(0.5f * hb * (1.0f + tanhf(u)));
    }
    lds[L_HA + lane] = hv;
  }

  constexpr float C2 = 0.08838834764831845f * 1.4426950408889634f;
  float m = -INFINITY, l = 0.f;
  float acc[8];
#pragma unroll
  for (int k = 0; k < 8; ++k) acc[k] = 0.f;

  u32x4 wor[10], w2r[5];

#define QK_PART(BUF)                                                  \
  float p_[4];                                                        \
  {                                                                   \
    float s_[4];                                                      \
    _Pragma("unroll") for (int t = 0; t < 4; ++t) {                   \
      float d = 0.f;                                                  \
      _Pragma("unroll") for (int k = 0; k < 4; ++k) d = dot2(BUF.k[t][k], q[k], d); \
      s_[t] = d;                                                      \
    }                                                                 \
    _Pragma("unroll") for (int t = 0; t < 4; ++t) s_[t] = row16_sum(s_[t]) * C2; \
    const float mn = fmaxf(fmaxf(m, fmaxf(s_[0], s_[1])), fmaxf(s_[2], s_[3])); \
    const float alpha = __builtin_amdgcn_exp2f(m - mn);               \
    m = mn;                                                           \
    _Pragma("unroll") for (int t = 0; t < 4; ++t) p_[t] = __builtin_amdgcn_exp2f(s_[t] - mn); \
    l = fmaf(l, alpha, (p_[0] + p_[1]) + (p_[2] + p_[3]));            \
    _Pragma("unroll") for (int k = 0; k < 8; ++k) acc[k] *= alpha;    \
  }
#define PV_PART(BUF)                                                  \
  _Pragma("unroll") for (int t = 0; t < 4; ++t) {                     \
    _Pragma("unroll") for (int k = 0; k < 4; ++k) {                   \
      acc[2 * k] = fmaf(p_[t], bf_lo(BUF.v[t][k]), acc[2 * k]);       \
      acc[2 * k + 1] = fmaf(p_[t], bf_hi(BUF.v[t][k]), acc[2 * k + 1]); \
    }                                                                 \
  }

  const u32x4* kq = kp + 2 * CH_STRIDE;
  const u32x4* vq = vp + 2 * CH_STRIDE;
  for (int it = 0; it < NCHUNK - 2; it += 2) {
    {
      QK_PART(A)
#pragma unroll
      for (int t = 0; t < 4; ++t) A.k[t] = ld_kv(kq + t * T_STRIDE);
      __builtin_amdgcn_sched_barrier(0);
      PV_PART(A)
#pragma unroll
      for (int t = 0; t < 4; ++t) A.v[t] = ld_kv(vq + t * T_STRIDE);
      __builtin_amdgcn_sched_barrier(0);
    }
    {
      QK_PART(Bf)
#pragma unroll
      for (int t = 0; t < 4; ++t) Bf.k[t] = ld_kv(kq + CH_STRIDE + t * T_STRIDE);
      __builtin_amdgcn_sched_barrier(0);
      PV_PART(Bf)
#pragma unroll
      for (int t = 0; t < 4; ++t) Bf.v[t] = ld_kv(vq + CH_STRIDE + t * T_STRIDE);
      __builtin_amdgcn_sched_barrier(0);
    }
    kq += 2 * CH_STRIDE;
    vq += 2 * CH_STRIDE;
  }
  const unsigned short* wop = wo + (size_t)(h * DH + 8 * w + r) * DM + c * 8;
  const int f2 = 4 * w + r;
  const unsigned short* w2p = w2 + (size_t)(FS * h + min(f2, FS - 1)) * DM + c * 8;
  {
    QK_PART(A)
#pragma unroll
    for (int i = 0; i < 4; ++i) wor[i] = *reinterpret_cast<const u32x4*>(wop + i * 128);
    __builtin_amdgcn_sched_barrier(0);
    PV_PART(A)
    wor[4] = *reinterpret_cast<const u32x4*>(wop + 4 * 128);
#pragma unroll
    for (int i = 0; i < 3; ++i) wor[5 + i] = *reinterpret_cast<const u32x4*>(wop + 4 * DM + i * 128);
    __builtin_amdgcn_sched_barrier(0);
  }
  {
    QK_PART(Bf)
#pragma unroll
    for (int i = 3; i < 5; ++i) wor[5 + i] = *reinterpret_cast<const u32x4*>(wop + 4 * DM + i * 128);
#pragma unroll
    for (int i = 0; i < 2; ++i) w2r[i] = *reinterpret_cast<const u32x4*>(w2p + i * 128);
    __builtin_amdgcn_sched_barrier(0);
    PV_PART(Bf)
#pragma unroll
    for (int i = 2; i < 5; ++i) w2r[i] = *reinterpret_cast<const u32x4*>(w2p + i * 128);
    __builtin_amdgcn_sched_barrier(0);
  }
#undef QK_PART
#undef PV_PART

  {
    const float mw = xrow_max(m);
    const float sc = __builtin_amdgcn_exp2f(m - mw);
    const float lw = xrow_sum(l * sc);
#pragma unroll
    for (int k = 0; k < 8; ++k) acc[k] = xrow_sum(acc[k] * sc);
    float* rec = &lds[L_MG + w * MG_REC];
    if (r == 0) {
      *reinterpret_cast<f32x4*>(rec + 4 + c * 8) = f32x4{acc[0], acc[1], acc[2], acc[3]};
      *reinterpret_cast<f32x4*>(rec + 4 + c * 8 + 4) = f32x4{acc[4], acc[5], acc[6], acc[7]};
      if (c == 0) { rec[0] = mw; rec[1] = lw; }
    }
  }
  __syncthreads();
  if (tid < DH) {
    float M = -INFINITY;
#pragma unroll
    for (int ww = 0; ww < NW; ++ww) M = fmaxf(M, lds[L_MG + ww * MG_REC]);
    float L = 0.f, o = 0.f;
#pragma unroll
    for (int ww = 0; ww < NW; ++ww) {
      const float* rec = &lds[L_MG + ww * MG_REC];
      const float wt = __builtin_amdgcn_exp2f(rec[0] - M);
      L = fmaf(wt, rec[1], L);
      o = fmaf(wt, rec[4 + tid], o);
    }
    lds[L_O + tid] = rbf(o / L);
  }
  __syncthreads();
  {
    const float o0 = lds[L_O + 8 * w + r], o1 = lds[L_O + 8 * w + 4 + r];
    const float hv = lds[L_HA + min(f2, 63)];
#pragma unroll
    for (int i = 0; i < 5; ++i) {
      float sv[8], mv[8];
#pragma unroll
      for (int k = 0; k < 4; ++k) {
        sv[2 * k] = fmaf(o1, bf_lo(wor[5 + i][k]), o0 * bf_lo(wor[i][k]));
        sv[2 * k + 1] = fmaf(o1, bf_hi(wor[5 + i][k]), o0 * bf_hi(wor[i][k]));
        mv[2 * k] = hv * bf_lo(w2r[i][k]);
        mv[2 * k + 1] = hv * bf_hi(w2r[i][k]);
      }
#pragma unroll
      for (int k = 0; k < 8; ++k) { sv[k] = xrow_sum(sv[k]); mv[k] = xrow_sum(mv[k]); }
      if (r == 0) {
        float* sp = &lds[L_SP + w * DM + (16 * i + c) * 8];
        *reinterpret_cast<f32x4*>(sp) = f32x4{sv[0], sv[1], sv[2], sv[3]};
        *reinterpret_cast<f32x4*>(sp + 4) = f32x4{sv[4], sv[5], sv[6], sv[7]};
      } else if (r == 1) {
        float* mp = &lds[L_MP + w * DM + (16 * i + c) * 8];
        *reinterpret_cast<f32x4*>(mp) = f32x4{mv[0], mv[1], mv[2], mv[3]};
        *reinterpret_cast<f32x4*>(mp + 4) = f32x4{mv[4], mv[5], mv[6], mv[7]};
      }
    }
  }
  __syncthreads();
  if (tid < 2 * (DM / 4)) {
    const int which = tid / (DM / 4), n4 = tid % (DM / 4);
    const float* src = &lds[(which ? L_MP : L_SP) + n4 * 4];
    f32x4 s = {0.f, 0.f, 0.f, 0.f};
#pragma unroll
    for (int ww = 0; ww < NW; ++ww) s += *reinterpret_cast<const f32x4*>(src + ww * DM);
    float* dst = (which ? mpart : spart) + (size_t)(b * NH + h) * DM + n4 * 4;
    *reinterpret_cast<f32x4*>(dst) = s;
  }
}

__global__ void __launch_bounds__(256) final_kernel(const unsigned short* __restrict__ x, const unsigned short* __restrict__ b2,
                                                    const float* __restrict__ spart, const float* __restrict__ mpart,
                                                    unsigned short* __restrict__ out) {
  __shared__ __attribute__((aligned(16))) float red[2][16][64];
  const int b = blockIdx.x / 10, seg = blockIdx.x % 10;
  const int tid = threadIdx.x, cq = tid & 15, g = tid >> 4;
  const size_t base = (size_t)(b * NH + 2 * g) * DM + seg * 64 + cq * 4;
  const f32x4 s0 = *reinterpret_cast<const f32x4*>(spart + base);
  const f32x4 s1 = *reinterpret_cast<const f32x4*>(spart + base + DM);
  const f32x4 m0 = *reinterpret_cast<const f32x4*>(mpart + base);
  const f32x4 m1 = *reinterpret_cast<const f32x4*>(mpart + base + DM);
  *reinterpret_cast<f32x4*>(&red[0][g][cq * 4]) = s0 + s1;
  *reinterpret_cast<f32x4*>(&red[1][g][cq * 4]) = m0 + m1;
  __syncthreads();
  if (tid < 64) {
    float s = 0.f, mm = 0.f;
#pragma unroll
    for (int k = 0; k < 16; ++k) { s += red[0][k][tid]; mm += red[1][k][tid]; }
    const int n = seg * 64 + tid;
    const float sb = rbf(s);
    const float mb = rbf(rbf(mm) + bf1(b2[n]));
    const float gt = rbf(mb / (1.0f + __expf(-mb)));
    const float res = bf1(x[b * DM + n]) + rbf(sb * gt);
    out[b * DM + n] = tobf(res);
  }
}

}

extern "C" void kernel_launch(void* const* d_in, const int* in_sizes, int n_in, void* d_out, int out_size, void* d_ws,
                              size_t ws_size, hipStream_t stream) {
  const unsigned short* x = (const unsigned short*)d_in[0];
  const unsigned short* kc = (const unsigned short*)d_in[1];
  const unsigned short* vc = (const unsigned short*)d_in[2];
  const unsigned short* wq = (const unsigned short*)d_in[3];
  const unsigned short* wo = (const unsigned short*)d_in[4];
  const unsigned short* w1 = (const unsigned short*)d_in[6];
  const unsigned short* b1 = (const unsigned short*)d_in[7];
  const unsigned short* w2 = (const unsigned short*)d_in[8];
  const unsigned short* b2 = (const unsigned short*)d_in[9];
  float* spart = (float*)d_ws;
  float* mpart = spart + (size_t)NB * NH * DM;
  fused_attn_kernel<<<NB * NH, NT, 0, stream>>>(x, kc, vc, wq, wo, w1, b1, w2, spart, mpart);
  final_kernel<<<NB * 10, 256, 0, stream>>>(x, b2, spart, mpart, (unsigned short*)d_out);
}
```

```cpp
#include <hip/hip_runtime.h>
#include <hip/hip_bf16.h>
#include <stdint.h>

#define STAMP(k) do { } while (0)

namespace {

constexpr int NB = 8, DM = 640, NH = 32, SK = 4096, DH = 128, FF = 1728;
constexpr int NW = 8;
constexpr int NT = NW * 64;
constexpr int FS = FF / NH;
constexpr int ROWS_W = DM / NW;
constexpr int NSC = SK / 64;

typedef unsigned u32x4 __attribute__((ext_vector_type(4)));
typedef float f32x4 __attribute__((ext_vector_type(4)));
typedef __bf16 bf16x2 __attribute__((ext_vector_type(2)));
typedef __bf16 bf16x8 __attribute__((ext_vector_type(8)));

__device__ __forceinline__ float bf_lo(unsigned u) { return __builtin_bit_cast(float, u << 16); }
__device__ __forceinline__ float bf_hi(unsigned u) { return __builtin_bit_cast(float, u & 0xffff0000u); }
__device__ __forceinline__ float bf1(unsigned short u) { return __builtin_bit_cast(float, (unsigned)u << 16); }
__device__ __forceinline__ float rbf(float x) { return (float)(__bf16)x; }
__device__ __forceinline__ unsigned short tobf(float x) { return __builtin_bit_cast(unsigned short, (__bf16)x); }
__device__ __forceinline__ unsigned pack2(float lo, float hi) { return (unsigned)tobf(lo) | ((unsigned)tobf(hi) << 16); }

__device__ __forceinline__ float dot2(unsigned k, unsigned q, float acc) {
  return __builtin_amdgcn_fdot2_f32_bf16(__builtin_bit_cast(bf16x2, k), __builtin_bit_cast(bf16x2, q), acc, false);
}
template <int CTRL> __device__ __forceinline__ float dpp(float x) {
  return __builtin_bit_cast(float, __builtin_amdgcn_mov_dpp(__builtin_bit_cast(int, x), CTRL, 0xf, 0xf, true));
}
__device__ __forceinline__ float row16_sum(float x) {
  x += dpp<0xB1>(x);
  x += dpp<0x4E>(x);
  x += dpp<0x141>(x);
  x += dpp<0x140>(x);
  return x;
}
__device__ __forceinline__ float xrow_sum(float x) {
  auto s = __builtin_amdgcn_permlane16_swap(__float_as_uint(x), __float_as_uint(x), false, false);
  x = __uint_as_float(s[0]) + __uint_as_float(s[1]);
  auto t = __builtin_amdgcn_permlane32_swap(__float_as_uint(x), __float_as_uint(x), false, false);
  return __uint_as_float(t[0]) + __uint_as_float(t[1]);
}
__device__ __forceinline__ float xrow_max(float x) {
  auto s = __builtin_amdgcn_permlane16_swap(__float_as_uint(x), __float_as_uint(x), false, false);
  x = fmaxf(__uint_as_float(s[0]), __uint_as_float(s[1]));
  auto t = __builtin_amdgcn_permlane32_swap(__float_as_uint(x), __float_as_uint(x), false, false);
  return fmaxf(__uint_as_float(t[0]), __uint_as_float(t[1]));
}
__device__ __forceinline__ float wave_sum(float x) { return xrow_sum(row16_sum(x)); }

__device__ __forceinline__ u32x4 ld_nt(const char* p) { return __builtin_nontemporal_load(reinterpret_cast<const u32x4*>(p)); }
__device__ __forceinline__ u32x4 ld_w(const char* p) { return *reinterpret_cast<const u32x4*>(p); }

__device__ __forceinline__ f32x4 gemv_mfma(u32x4 a, unsigned xdup, u32x4 msk, f32x4 c) {
  const u32x4 bsel = {xdup & msk[0], xdup & msk[1], xdup & msk[2], xdup & msk[3]};
  return __builtin_amdgcn_mfma_f32_16x16x32_bf16(__builtin_bit_cast(bf16x8, a), __builtin_bit_cast(bf16x8, bsel), c, 0, 0, 0);
}

constexpr int L_XQ = 0;
constexpr int L_X1 = L_XQ + NW * 80;
constexpr int L_QP = L_X1 + NW * 96;
constexpr int L_HP = L_QP + NW * DH;
constexpr int L_HA = L_HP + NW * 128;
constexpr int L_MG = L_HA + 64;
constexpr int MG_REC = DH + 4;
constexpr int L_PP = L_MG + NW * MG_REC;
constexpr int L_CNT = L_PP + NW * 10 * 128;
constexpr int L_END = L_CNT + 4;

struct Slot { u32x4 k[4]; u32x4 v[4]; };

__global__ void __launch_bounds__(NT, 2) fused_attn_kernel(
    const unsigned short* __restrict__ x, const char* __restrict__ kc, const char* __restrict__ vc,
    const char* __restrict__ wq, const char* __restrict__ wo, const char* __restrict__ w1,
    const unsigned short* __restrict__ b1, const char* __restrict__ w2, float* __restrict__ spart) {
  __shared__ __attribute__((aligned(16))) unsigned lds[L_END];
  float* ldsf = reinterpret_cast<float*>(lds);
  const int tid = threadIdx.x;
  const unsigned lane = tid & 63;
  const int w = __builtin_amdgcn_readfirstlane(tid >> 6);
  const unsigned r = lane >> 4, c = lane & 15;
  const int bid = blockIdx.x;
  const int h = bid & 31, b = bid >> 5;
  const int js = (h & 7) * 4 + (h >> 3);
#define STAMP_BASE 0
  STAMP(0);

  const unsigned short* xb = x + b * DM;
  const u32x4 xa = *reinterpret_cast<const u32x4*>(xb + lane * 8);
  const unsigned xt = *reinterpret_cast<const unsigned*>(xb + 512 + lane * 2);
  const unsigned short xo0 = xb[ROWS_W * w + lane];
  const unsigned short xo1 = xb[ROWS_W * w + 64 + c];
  __builtin_amdgcn_sched_barrier(0);

  u32x4 wqr[20];
  {
    const char* p = wq + (size_t)(ROWS_W * w) * (NH * DH * 2) + h * (DH * 2);
    const unsigned lo = r * (NH * DH * 2) + c * 16;
#pragma unroll
    for (int i = 0; i < 20; ++i) wqr[i] = ld_w(p + (size_t)(4 * i) * (NH * DH * 2) + lo);
  }
  u32x4 w1r[10];
  const unsigned r8 = lane >> 3, c8 = lane & 7;
  const int chunk0 = (FS * js) >> 3;
  {
    const unsigned ch = min((unsigned)chunk0 + c8, (unsigned)(FF / 8 - 1));
    const char* p = w1 + (size_t)(ROWS_W * w) * (FF * 2);
    const unsigned lo = r8 * (FF * 2) + ch * 16;
#pragma unroll
    for (int i = 0; i < 10; ++i) w1r[i] = ld_w(p + (size_t)(8 * i) * (FF * 2) + lo);
  }
  const unsigned short b1u = b1[FS * js + min(lane, (unsigned)(FS - 1))];
  __builtin_amdgcn_sched_barrier(0);

  const char* kb = kc + (size_t)bid * (SK * DH * 2);
  const char* vb = vc + (size_t)bid * (SK * DH * 2);
  const unsigned lo16 = lane * 16;
  Slot S[4];
#define LOAD_K(d, sc) _Pragma("unroll") for (int t = 0; t < 4; ++t) S[d].k[t] = ld_nt(kb + (size_t)(sc) * 16384 + (d) * 4096 + t * 1024 + lo16);
#define LOAD_V(d, sc) _Pragma("unroll") for (int t = 0; t < 4; ++t) S[d].v[t] = ld_nt(vb + (size_t)(sc) * 16384 + (d) * 4096 + t * 1024 + lo16);
  LOAD_K(0, w) LOAD_V(0, w) LOAD_K(1, w) LOAD_V(1, w)
  __builtin_amdgcn_sched_barrier(0);

  if (tid == 0) lds[L_CNT] = 2 * NW;
  u32x4 msk8, msk16;
#pragma unroll
  for (int k = 0; k < 4; ++k) {
    const unsigned m16 = (c8 == 2u * k) ? 0x0000ffffu : ((c8 == 2u * k + 1) ? 0xffff0000u : 0u);
    msk16[k] = m16;
    msk8[k] = (c < 8) ? m16 : 0u;
  }

  {
    float xv[10];
#pragma unroll
    for (int k = 0; k < 4; ++k) { xv[2 * k] = bf_lo(xa[k]); xv[2 * k + 1] = bf_hi(xa[k]); }
    xv[8] = bf_lo(xt); xv[9] = bf_hi(xt);
    float s = 0.f;
#pragma unroll
    for (int k = 0; k < 10; ++k) s += xv[k];
    const float mean = wave_sum(s) * (1.0f / DM);
    float ss = 0.f;
#pragma unroll
    for (int k = 0; k < 10; ++k) { const float d = xv[k] - mean; ss = fmaf(d, d, ss); }
    const float var = wave_sum(ss) * (1.0f / DM);
    const float mb = rbf(mean);
    const float sd = rbf(sqrtf(rbf(rbf(var) + 1e-5f)));
    const unsigned n0 = tobf(rbf(bf1(xo0) - mb) / sd);
    const unsigned n1 = tobf(rbf(bf1(xo1) - mb) / sd);
    const unsigned d0 = n0 | (n0 << 16), d1 = n1 | (n1 << 16);
    lds[L_XQ + w * 80 + (lane & 3) * 20 + (lane >> 2)] = d0;
    lds[L_X1 + w * 96 + (lane & 7) * 12 + (lane >> 3)] = d0;
    if (lane < 16) {
      lds[L_XQ + w * 80 + (lane & 3) * 20 + 16 + (lane >> 2)] = d1;
      lds[L_X1 + w * 96 + (lane & 7) * 12 + 8 + (lane >> 3)] = d1;
    }
  }
  __builtin_amdgcn_fence(__ATOMIC_RELEASE, "wavefront");
  __builtin_amdgcn_wave_barrier();
  __builtin_amdgcn_fence(__ATOMIC_ACQUIRE, "wavefront");

  {
    f32x4 a0 = {0.f, 0.f, 0.f, 0.f}, a1 = {0.f, 0.f, 0.f, 0.f};
#pragma unroll
    for (int i4 = 0; i4 < 5; ++i4) {
      const u32x4 xd = *reinterpret_cast<const u32x4*>(&lds[L_XQ + w * 80 + r * 20 + i4 * 4]);
      a0 = gemv_mfma(wqr[4 * i4 + 0], xd[0], msk8, a0);
      a1 = gemv_mfma(wqr[4 * i4 + 1], xd[1], msk8, a1);
      a0 = gemv_mfma(wqr[4 * i4 + 2], xd[2], msk8, a0);
      a1 = gemv_mfma(wqr[4 * i4 + 3], xd[3], msk8, a1);
    }
    a0 += a1;
    if (c < 8) {
#pragma unroll
      for (int g = 0; g < 4; ++g) ldsf[L_QP + w * DH + 8 * (4 * r + g) + c] = a0[g];
    }
  }
  __builtin_amdgcn_sched_barrier(0);
  LOAD_K(2, w) LOAD_V(2, w) LOAD_K(3, w) LOAD_V(3, w)
  __builtin_amdgcn_sched_barrier(0);
  {
    f32x4 a0 = {0.f, 0.f, 0.f, 0.f}, a1 = {0.f, 0.f, 0.f, 0.f};
    const u32x4 x0 = *reinterpret_cast<const u32x4*>(&lds[L_X1 + w * 96 + r8 * 12]);
    const u32x4 x1 = *reinterpret_cast<const u32x4*>(&lds[L_X1 + w * 96 + r8 * 12 + 4]);
    const u32x4 x2 = *reinterpret_cast<const u32x4*>(&lds[L_X1 + w * 96 + r8 * 12 + 8]);
#pragma unroll
    for (int i = 0; i < 4; ++i) { a0 = gemv_mfma(w1r[i], x0[i], msk16, a0); a1 = gemv_mfma(w1r[4 + i], x1[i], msk16, a1); }
    a0 = gemv_mfma(w1r[8], x2[0], msk16, a0);
    a1 = gemv_mfma(w1r[9], x2[1], msk16, a1);
    a0 += a1;
    if ((r >> 1) == (c >> 3)) {
#pragma unroll
      for (int g = 0; g < 4; ++g) ldsf[L_HP + w * 128 + (r >> 1) * 64 + 8 * (4 * (r & 1) + g) + c8] = a0[g];
    }
  }
  __syncthreads();
  u32x4 q;
  {
    const float* qp = &ldsf[L_QP + (2 * r) * DH + c * 8];
    f32x4 s0 = *reinterpret_cast<const f32x4*>(qp) + *reinterpret_cast<const f32x4*>(qp + DH);
    f32x4 s1 = *reinterpret_cast<const f32x4*>(qp + 4) + *reinterpret_cast<const f32x4*>(qp + DH + 4);
#pragma unroll
    for (int k = 0; k < 4; ++k) { s0[k] = xrow_sum(s0[k]); s1[k] = xrow_sum(s1[k]); }
    q[0] = pack2(s0[0], s0[1]); q[1] = pack2(s0[2], s0[3]); q[2] = pack2(s1[0], s1[1]); q[3] = pack2(s1[2], s1[3]);
  }
  asm volatile("" : "+v"(q));
  STAMP(1);
  if (w == NW - 1) {
    const unsigned off = FS * js - chunk0 * 8;
    unsigned hd = 0u;
    if (lane < FS) {
      float s = 0.f;
#pragma unroll
      for (int ww = 0; ww < 2 * NW; ++ww) s += ldsf[L_HP + ww * 64 + off + lane];
      const float hb = rbf(rbf(s) + bf1(b1u));
      const float u2 = 1.5957691216057308f * (hb + 0.044715f * hb * hb * hb);
      const unsigned hv = tobf(hb / (1.0f + __expf(-u2)));
      hd = hv | (hv << 16);
    }
    lds[L_HA + lane] = hd;
  }

  constexpr float C2 = 0.08838834764831845f * 1.4426950408889634f;
  float m = -INFINITY, l = 0.f;
  float acc[8];
#pragma unroll
  for (int k = 0; k < 8; ++k) acc[k] = 0.f;

#define QK_PART(d)                                                    \
  float p_[4];                                                        \
  {                                                                   \
    float s_[4];                                                      \
    _Pragma("unroll") for (int t = 0; t < 4; ++t) {                   \
      float dd = 0.f;                                                 \
      _Pragma("unroll") for (int k = 0; k < 4; ++k) dd = dot2(S[d].k[t][k], q[k], dd); \
      s_[t] = dd;                                                     \
    }                                                                 \
    _Pragma("unroll") for (int t = 0; t < 4; ++t) s_[t] = row16_sum(s_[t]) * C2; \
    const float mn = fmaxf(fmaxf(m, fmaxf(s_[0], s_[1])), fmaxf(s_[2], s_[3])); \
    const float alpha = __builtin_amdgcn_exp2f(m - mn);               \
    m = mn;                                                           \
    _Pragma("unroll") for (int t = 0; t < 4; ++t) p_[t] = __builtin_amdgcn_exp2f(s_[t] - mn); \
    l = fmaf(l, alpha, (p_[0] + p_[1]) + (p_[2] + p_[3]));            \
    _Pragma("unroll") for (int k = 0; k < 8; ++k) acc[k] *= alpha;    \
  }
#define PV_PART(d)                                                    \
  _Pragma("unroll") for (int t = 0; t < 4; ++t) {                     \
    _Pragma("unroll") for (int k = 0; k < 4; ++k) {                   \
      acc[2 * k] = fmaf(p_[t], bf_lo(S[d].v[t][k]), acc[2 * k]);      \
      acc[2 * k + 1] = fmaf(p_[t], bf_hi(S[d].v[t][k]), acc[2 * k + 1]); \
    }                                                                 \
  }                                                                   \
  asm volatile("" : "+v"(acc[0]), "+v"(acc[1]), "+v"(acc[2]), "+v"(acc[3]), "+v"(acc[4]), "+v"(acc[5]), "+v"(acc[6]), "+v"(acc[7]));
#define STEP(d)                                                       \
  {                                                                   \
    QK_PART(d)                                                        \
    LOAD_K(d, nxt)                                                    \
    __builtin_amdgcn_sched_barrier(0);                                \
    PV_PART(d)                                                        \
    LOAD_V(d, nxt)                                                    \
    __builtin_amdgcn_sched_barrier(0);                                \
  }

  int nxt = NW + w;
  while (nxt < NSC) {
    unsigned cl = 0;
    if (lane == 0) cl = atomicAdd(&lds[L_CNT], 1u);
    STEP(0) STEP(1) STEP(2) STEP(3)
    nxt = __builtin_amdgcn_readfirstlane(cl);
  }
  u32x4 wor[20], w2r[10];
  const char* wop = wo + (size_t)(h * DH + 16 * w) * (DM * 2);
  const unsigned wlo = r * (DM * 2) + c * 16;
  const char* w2p = w2 + (size_t)(FS * js) * (DM * 2);
  const unsigned w2lo0 = min(8u * w + r, (unsigned)(FS - 1)) * (DM * 2) + c * 16;
  const unsigned w2lo1 = min(8u * w + 4 + r, (unsigned)(FS - 1)) * (DM * 2) + c * 16;
#define LOAD_WO(u) wor[u] = ld_w(wop + ((u) / 5) * (4 * DM * 2) + ((u) % 5) * 256 + wlo);
#define LOAD_W2(u) w2r[u] = ld_w(w2p + ((u) % 5) * 256 + ((u) < 5 ? w2lo0 : w2lo1));
  {
    QK_PART(0)
    LOAD_WO(0) LOAD_WO(1) LOAD_WO(2) LOAD_WO(3)
    __builtin_amdgcn_sched_barrier(0);
    PV_PART(0)
    LOAD_WO(4) LOAD_WO(5) LOAD_WO(6) LOAD_WO(7)
    __builtin_amdgcn_sched_barrier(0);
  }
  {
    QK_PART(1)
    LOAD_WO(8) LOAD_WO(9) LOAD_WO(10) LOAD_WO(11)
    __builtin_amdgcn_sched_barrier(0);
    PV_PART(1)
    LOAD_WO(12) LOAD_WO(13) LOAD_WO(14) LOAD_WO(15)
    __builtin_amdgcn_sched_barrier(0);
  }
  {
    QK_PART(2)
    LOAD_WO(16) LOAD_WO(17) LOAD_WO(18) LOAD_WO(19)
    __builtin_amdgcn_sched_barrier(0);
    PV_PART(2)
    LOAD_W2(0) LOAD_W2(1) LOAD_W2(2) LOAD_W2(3)
    __builtin_amdgcn_sched_barrier(0);
  }
  {
    QK_PART(3)
    LOAD_W2(4) LOAD_W2(5) LOAD_W2(6) LOAD_W2(7) LOAD_W2(8) LOAD_W2(9)
    __builtin_amdgcn_sched_barrier(0);
    PV_PART(3)
  }
  STAMP(2);

  {
    const float mw = xrow_max(m);
    const float sc = __builtin_amdgcn_exp2f(m - mw);
    const float lw = xrow_sum(l * sc);
#pragma unroll
    for (int k = 0; k < 8; ++k) acc[k] = xrow_sum(acc[k] * sc);
    float* rec = &ldsf[L_MG + w * MG_REC];
    if (r == 0) {
      *reinterpret_cast<f32x4*>(rec + 4 + c * 8) = f32x4{acc[0], acc[1], acc[2], acc[3]};
      *reinterpret_cast<f32x4*>(rec + 4 + c * 8 + 4) = f32x4{acc[4], acc[5], acc[6], acc[7]};
      if (c == 0) { rec[0] = mw; rec[1] = lw; }
    }
  }
  __syncthreads();
  STAMP(3);
  unsigned od[4];
  {
    float mv[NW], lv[NW], av[NW];
#pragma unroll
    for (int ww = 0; ww < NW; ++ww) {
      const float* rec = &ldsf[L_MG + ww * MG_REC];
      mv[ww] = rec[0]; lv[ww] = rec[1]; av[ww] = rec[4 + 16 * w + c];
    }
    float M = mv[0];
#pragma unroll
    for (int ww = 1; ww < NW; ++ww) M = fmaxf(M, mv[ww]);
    float L = 0.f, o = 0.f;
#pragma unroll
    for (int ww = 0; ww < NW; ++ww) {
      const float wt = __builtin_amdgcn_exp2f(mv[ww] - M);
      L = fmaf(wt, lv[ww], L);
      o = fmaf(wt, av[ww], o);
    }
    const unsigned ob = tobf(o / L);
    const unsigned odup = ob | (ob << 16);
#pragma unroll
    for (int g = 0; g < 4; ++g) od[g] = __builtin_amdgcn_ds_bpermute((int)((4 * g + r) * 4), (int)odup);
  }
  const unsigned hd0 = lds[L_HA + ((8 * w + r) & 63)];
  const unsigned hd1 = lds[L_HA + ((8 * w + 4 + r) & 63)];
#pragma unroll
  for (int s = 0; s < 5; ++s) {
    f32x4 a = {0.f, 0.f, 0.f, 0.f}, mm = {0.f, 0.f, 0.f, 0.f};
#pragma unroll
    for (int g = 0; g < 4; ++g) a = gemv_mfma(wor[5 * g + s], od[g], msk8, a);
    mm = gemv_mfma(w2r[s], hd0, msk8, mm);
    mm = gemv_mfma(w2r[5 + s], hd1, msk8, mm);
    if (c < 8) {
      *reinterpret_cast<f32x4*>(&ldsf[L_PP + ((w * 10 + s) * 32 + r * 8 + c) * 4]) = a;
      *reinterpret_cast<f32x4*>(&ldsf[L_PP + ((w * 10 + 5 + s) * 32 + r * 8 + c) * 4]) = mm;
    }
  }
  __syncthreads();
  STAMP(4);
  if (tid < 320) {
    f32x4 s = {0.f, 0.f, 0.f, 0.f};
#pragma unroll
    for (int ww = 0; ww < NW; ++ww) s += *reinterpret_cast<const f32x4*>(&ldsf[L_PP + (ww * 320 + tid) * 4]);
    *reinterpret_cast<f32x4*>(spart + (size_t)bid * 1280 + tid * 4) = s;
  }
  STAMP(5);
#undef STAMP_BASE
}

__global__ void __launch_bounds__(256) final_kernel(const unsigned short* __restrict__ x, const unsigned short* __restrict__ b2,
                                                    const float* __restrict__ spart, unsigned short* __restrict__ out) {
  __shared__ __attribute__((aligned(16))) float red[2][16][64];
  const int b = blockIdx.x / 10, seg = blockIdx.x % 10;
#define STAMP_BASE 256
  STAMP(0);
  const int tid = threadIdx.x, cq = tid & 15, g = tid >> 4;
  const size_t base = (size_t)(b * NH + 2 * g) * 1280 + seg * 64 + cq * 4;
  const f32x4 s0 = *reinterpret_cast<const f32x4*>(spart + base);
  const f32x4 s1 = *reinterpret_cast<const f32x4*>(spart + base + 1280);
  const f32x4 m0 = *reinterpret_cast<const f32x4*>(spart + base + 640);
  const f32x4 m1 = *reinterpret_cast<const f32x4*>(spart + base + 1280 + 640);
  const int p = seg * 64 + (tid & 63);
  const int n = (p >> 7) * 128 + 8 * (4 * ((p >> 5) & 3) + (p & 3)) + ((p >> 2) & 7);
  const float b2v = bf1(b2[n]);
  const float xv = bf1(x[b * DM + n]);
  *reinterpret_cast<f32x4*>(&red[0][g][cq * 4]) = s0 + s1;
  *reinterpret_cast<f32x4*>(&red[1][g][cq * 4]) = m0 + m1;
  __syncthreads();
  if (tid < 64) {
    float s = 0.f, mm = 0.f;
#pragma unroll
    for (int k = 0; k < 16; ++k) { s += red[0][k][tid]; mm += red[1][k][tid]; }
    const float sb = rbf(s);
    const float mb = rbf(rbf(mm) + b2v);
    const float gt = rbf(mb / (1.0f + __expf(-mb)));
    const float res = xv + rbf(sb * gt);
    out[b * DM + n] = tobf(res);
  }
  STAMP(1);
#undef STAMP_BASE
}

}

extern "C" void kernel_launch(void* const* d_in, const int* in_sizes, int n_in, void* d_out, int out_size, void* d_ws,
                              size_t ws_size, hipStream_t stream) {
  const unsigned short* x = (const unsigned short*)d_in[0];
  const char* kc = (const char*)d_in[1];
  const char* vc = (const char*)d_in[2];
  const char* wq = (const char*)d_in[3];
  const char* wo = (const char*)d_in[4];
  const char* w1 = (const char*)d_in[6];
  const unsigned short* b1 = (const unsigned short*)d_in[7];
  const char* w2 = (const char*)d_in[8];
  const unsigned short* b2 = (const unsigned short*)d_in[9];
  float* spart = (float*)d_ws;
  fused_attn_kernel<<<NB * NH, NT, 0, stream>>>(x, kc, vc, wq, wo, w1, b1, w2, spart);
  final_kernel<<<NB * 10, 256, 0, stream>>>(x, b2, spart, (unsigned short*)d_out);
}
```

```cpp
#include <hip/hip_runtime.h>
#include <hip/hip_bf16.h>
#include <stdint.h>

#define STAMP_DECL
#define STAMP(k) do { } while (0)
#define STAMP_FLUSH(base) do { } while (0)

#define V_WNT 1
#define V_SLP 16
#define V_BOOT 1

namespace {

constexpr int NB = 8, DM = 640, NH = 32, SK = 4096, DH = 128, FF = 1728;
constexpr int NW = 8;
constexpr int NT = NW * 64;
constexpr int NHLP = 4, NSTR = 4;
constexpr int FS = FF / NH;
constexpr int QROWS = DM / NHLP;
constexpr int NSC = SK / 64;
constexpr int NSC_DYN = V_BOOT ? NSC - 2 : NSC;

typedef unsigned u32x4 __attribute__((ext_vector_type(4)));
typedef float f32x4 __attribute__((ext_vector_type(4)));
typedef __bf16 bf16x2 __attribute__((ext_vector_type(2)));
typedef __bf16 bf16x8 __attribute__((ext_vector_type(8)));

__device__ __forceinline__ float bf_lo(unsigned u) { return __builtin_bit_cast(float, u << 16); }
__device__ __forceinline__ float bf_hi(unsigned u) { return __builtin_bit_cast(float, u & 0xffff0000u); }
__device__ __forceinline__ float bf1(unsigned short u) { return __builtin_bit_cast(float, (unsigned)u << 16); }
__device__ __forceinline__ float rbf(float x) { return (float)(__bf16)x; }
__device__ __forceinline__ unsigned short tobf(float x) { return __builtin_bit_cast(unsigned short, (__bf16)x); }
__device__ __forceinline__ unsigned pack2(float lo, float hi) { return (unsigned)tobf(lo) | ((unsigned)tobf(hi) << 16); }

__device__ __forceinline__ float dot2(unsigned k, unsigned q, float acc) {
  return __builtin_amdgcn_fdot2_f32_bf16(__builtin_bit_cast(bf16x2, k), __builtin_bit_cast(bf16x2, q), acc, false);
}
template <int CTRL> __device__ __forceinline__ float dpp(float x) {
  return __builtin_bit_cast(float, __builtin_amdgcn_mov_dpp(__builtin_bit_cast(int, x), CTRL, 0xf, 0xf, true));
}
__device__ __forceinline__ float row16_sum(float x) {
  x += dpp<0xB1>(x);
  x += dpp<0x4E>(x);
  x += dpp<0x141>(x);
  x += dpp<0x140>(x);
  return x;
}
__device__ __forceinline__ float xrow_sum(float x) {
  auto s = __builtin_amdgcn_permlane16_swap(__float_as_uint(x), __float_as_uint(x), false, false);
  x = __uint_as_float(s[0]) + __uint_as_float(s[1]);
  auto t = __builtin_amdgcn_permlane32_swap(__float_as_uint(x), __float_as_uint(x), false, false);
  return __uint_as_float(t[0]) + __uint_as_float(t[1]);
}
__device__ __forceinline__ float xrow_max(float x) {
  auto s = __builtin_amdgcn_permlane16_swap(__float_as_uint(x), __float_as_uint(x), false, false);
  x = fmaxf(__uint_as_float(s[0]), __uint_as_float(s[1]));
  auto t = __builtin_amdgcn_permlane32_swap(__float_as_uint(x), __float_as_uint(x), false, false);
  return fmaxf(__uint_as_float(t[0]), __uint_as_float(t[1]));
}
__device__ __forceinline__ float wave_sum(float x) { return xrow_sum(row16_sum(x)); }

__device__ __forceinline__ u32x4 ld_nt(const char* p) { return __builtin_nontemporal_load(reinterpret_cast<const u32x4*>(p)); }
__device__ __forceinline__ u32x4 ld_w(const char* p) { return __builtin_nontemporal_load(reinterpret_cast<const u32x4*>(p)); }

__device__ __forceinline__ f32x4 gemv_mfma(u32x4 a, unsigned xdup, u32x4 msk, f32x4 c) {
  const u32x4 bsel = {xdup & msk[0], xdup & msk[1], xdup & msk[2], xdup & msk[3]};
  return __builtin_amdgcn_mfma_f32_16x16x32_bf16(__builtin_bit_cast(bf16x8, a), __builtin_bit_cast(bf16x8, bsel), c, 0, 0, 0);
}

constexpr int L_XQ = 0;
constexpr int L_X1 = L_XQ + DM;
constexpr int L_QP = L_X1 + DM;
constexpr int L_HP = L_QP + NHLP * DH;
constexpr int L_HA = L_HP + NHLP * 128;
constexpr int L_MG = L_HA + 64;
constexpr int MG_REC = DH + 4;
constexpr int L_PP = L_MG + NSTR * MG_REC;
constexpr int L_CNT = L_PP + NHLP * 10 * 128;
constexpr int L_END = L_CNT + 4;

struct Slot { u32x4 k[4]; u32x4 v[4]; };

__global__ void __launch_bounds__(NT, 2) fused_attn_kernel(
    const unsigned short* __restrict__ x, const char* __restrict__ kc, const char* __restrict__ vc,
    const char* __restrict__ wq, const char* __restrict__ wo, const char* __restrict__ w1,
    const unsigned short* __restrict__ b1, const char* __restrict__ w2, float* __restrict__ spart) {
  __shared__ __attribute__((aligned(16))) unsigned lds[L_END];
  float* ldsf = reinterpret_cast<float*>(lds);
  const int tid = threadIdx.x;
  const unsigned lane = tid & 63;
  const int w = __builtin_amdgcn_readfirstlane(tid >> 6);
  const unsigned r = lane >> 4, c = lane & 15;
  const int bid = blockIdx.x;
  const int h = bid & 31, b = bid >> 5;
  STAMP_DECL
  STAMP(0);
#define SB __builtin_amdgcn_sched_barrier(0);
#define PACE SB __builtin_amdgcn_s_sleep(V_SLP); SB

  if (w >= NHLP) {
    const int j = w - NHLP;
    const char* kb = kc + (size_t)bid * (SK * DH * 2);
    const char* vb = vc + (size_t)bid * (SK * DH * 2);
    const unsigned lo16 = lane * 16;
#define SC_OFF(d, sc) ((size_t)((sc) >> 2) * 65536 + ((sc) & 3) * 4096 + (d) * 16384)
    Slot S[4];
#define LOAD_K(Sd, d, sc) _Pragma("unroll") for (int t = 0; t < 4; ++t) Sd.k[t] = ld_nt(kb + SC_OFF(d, sc) + t * 1024 + lo16);
#define LOAD_V(Sd, d, sc) _Pragma("unroll") for (int t = 0; t < 4; ++t) Sd.v[t] = ld_nt(vb + SC_OFF(d, sc) + t * 1024 + lo16);
    Slot Bt[2];
    LOAD_K(Bt[0], 2 * (j & 1), NSC - 2 + (j >> 1)) SB LOAD_V(Bt[0], 2 * (j & 1), NSC - 2 + (j >> 1)) SB
    LOAD_K(Bt[1], 2 * (j & 1) + 1, NSC - 2 + (j >> 1)) SB LOAD_V(Bt[1], 2 * (j & 1) + 1, NSC - 2 + (j >> 1)) SB
    LOAD_K(S[0], 0, j) SB LOAD_V(S[0], 0, j) SB LOAD_K(S[1], 1, j) SB LOAD_V(S[1], 1, j) SB
    LOAD_K(S[2], 2, j) SB LOAD_V(S[2], 2, j) SB LOAD_K(S[3], 3, j) SB LOAD_V(S[3], 3, j) SB
    STAMP(1);
    __syncthreads();
    STAMP(2);
    u32x4 q;
    {
      const float* qp = &ldsf[L_QP + r * DH + c * 8];
      f32x4 s0 = *reinterpret_cast<const f32x4*>(qp);
      f32x4 s1 = *reinterpret_cast<const f32x4*>(qp + 4);
#pragma unroll
      for (int k = 0; k < 4; ++k) { s0[k] = xrow_sum(s0[k]); s1[k] = xrow_sum(s1[k]); }
      q[0] = pack2(s0[0], s0[1]); q[1] = pack2(s0[2], s0[3]); q[2] = pack2(s1[0], s1[1]); q[3] = pack2(s1[2], s1[3]);
    }
    asm volatile("" : "+v"(q));
    STAMP(3);

    constexpr float C2 = 0.08838834764831845f * 1.4426950408889634f;
    float m = -INFINITY, l = 0.f;
    float acc[8];
#pragma unroll
    for (int k = 0; k < 8; ++k) acc[k] = 0.f;

#define QK_PART(Sd)                                                   \
  float p_[4];                                                        \
  {                                                                   \
    float s_[4];                                                      \
    _Pragma("unroll") for (int t = 0; t < 4; ++t) {                   \
      float dd = 0.f;                                                 \
      _Pragma("unroll") for (int k = 0; k < 4; ++k) dd = dot2(Sd.k[t][k], q[k], dd); \
      s_[t] = dd;                                                     \
    }                                                                 \
    _Pragma("unroll") for (int t = 0; t < 4; ++t) s_[t] = row16_sum(s_[t]) * C2; \
    const float mn = fmaxf(fmaxf(m, fmaxf(s_[0], s_[1])), fmaxf(s_[2], s_[3])); \
    const float alpha = __builtin_amdgcn_exp2f(m - mn);               \
    m = mn;                                                           \
    _Pragma("unroll") for (int t = 0; t < 4; ++t) p_[t] = __builtin_amdgcn_exp2f(s_[t] - mn); \
    l = fmaf(l, alpha, (p_[0] + p_[1]) + (p_[2] + p_[3]));            \
    _Pragma("unroll") for (int k = 0; k < 8; ++k) acc[k] *= alpha;    \
  }
#define PV_PART(Sd)                                                   \
  _Pragma("unroll") for (int t = 0; t < 4; ++t) {                     \
    _Pragma("unroll") for (int k = 0; k < 4; ++k) {                   \
      acc[2 * k] = fmaf(p_[t], bf_lo(Sd.v[t][k]), acc[2 * k]);        \
      acc[2 * k + 1] = fmaf(p_[t], bf_hi(Sd.v[t][k]), acc[2 * k + 1]); \
    }                                                                 \
  }                                                                   \
  asm volatile("" : "+v"(acc[0]), "+v"(acc[1]), "+v"(acc[2]), "+v"(acc[3]), "+v"(acc[4]), "+v"(acc[5]), "+v"(acc[6]), "+v"(acc[7]));
#define STEP(d) { QK_PART(S[d]) LOAD_K(S[d], d, nxt) SB PV_PART(S[d]) LOAD_V(S[d], d, nxt) SB }
#define STEP_LAST(Sd) { QK_PART(Sd) SB PV_PART(Sd) SB }

    STEP_LAST(Bt[0]) STEP_LAST(Bt[1])
    int nxt = NSTR + j;
    while (nxt < NSC_DYN) {
      unsigned cl = 0;
      if (lane == 0) cl = atomicAdd(&lds[L_CNT], 1u);
      STEP(0) STEP(1) STEP(2) STEP(3)
      nxt = __builtin_amdgcn_readfirstlane(cl);
    }
    STEP_LAST(S[0]) STEP_LAST(S[1]) STEP_LAST(S[2]) STEP_LAST(S[3])
    STAMP(4);
    {
      const float mw = xrow_max(m);
      const float sc = __builtin_amdgcn_exp2f(m - mw);
      const float lw = xrow_sum(l * sc);
#pragma unroll
      for (int k = 0; k < 8; ++k) acc[k] = xrow_sum(acc[k] * sc);
      float* rec = &ldsf[L_MG + j * MG_REC];
      if (r == 0) {
        *reinterpret_cast<f32x4*>(rec + 4 + c * 8) = f32x4{acc[0], acc[1], acc[2], acc[3]};
        *reinterpret_cast<f32x4*>(rec + 4 + c * 8 + 4) = f32x4{acc[4], acc[5], acc[6], acc[7]};
        if (c == 0) { rec[0] = mw; rec[1] = lw; }
      }
    }
    __syncthreads();
    STAMP(5);
    __syncthreads();
    STAMP(6);
  } else {
    __builtin_amdgcn_s_setprio(3);
    const int js = (h & 7) * 4 + (h >> 3);
    const unsigned r8 = lane >> 3, c8 = lane & 7;
    const int chunk0 = (FS * js) >> 3;
    const unsigned short* xb = x + b * DM;
    const u32x4 xa = *reinterpret_cast<const u32x4*>(xb + lane * 8);
    const unsigned xt = *reinterpret_cast<const unsigned*>(xb + 512 + lane * 2);
    const unsigned short xo0 = xb[QROWS * w + lane];
    const unsigned short xo1 = xb[QROWS * w + 64 + lane];
    const unsigned short xo2 = xb[QROWS * w + 128 + (lane & 31)];
    const unsigned short b1u = b1[FS * js + min(lane, (unsigned)(FS - 1))];
    SB
    u32x4 wr[40];
    {
      const char* p = wq + (size_t)(QROWS * w) * (NH * DH * 2) + h * (DH * 2);
      const unsigned lo = r * (NH * DH * 2) + c * 16;
#pragma unroll
      for (int i = 0; i < 40; ++i) {
        wr[i] = ld_w(p + (size_t)(4 * i) * (NH * DH * 2) + lo);
        if ((i & 3) == 0) { SB }
      }
    }
    SB
    if (tid == 0) { lds[L_CNT] = 2 * NSTR; lds[L_CNT + 1] = 0; lds[L_CNT + 2] = 0; }
    u32x4 msk8, msk16;
#pragma unroll
    for (int k = 0; k < 4; ++k) {
      const unsigned m16 = (c8 == 2u * k) ? 0x0000ffffu : ((c8 == 2u * k + 1) ? 0xffff0000u : 0u);
      msk16[k] = m16;
      msk8[k] = (c < 8) ? m16 : 0u;
    }
    {
      float xv[10];
#pragma unroll
      for (int k = 0; k < 4; ++k) { xv[2 * k] = bf_lo(xa[k]); xv[2 * k + 1] = bf_hi(xa[k]); }
      xv[8] = bf_lo(xt); xv[9] = bf_hi(xt);
      float sm = 0.f;
#pragma unroll
      for (int k = 0; k < 10; ++k) sm += xv[k];
      const float mean = wave_sum(sm) * (1.0f / DM);
      float ss = 0.f;
#pragma unroll
      for (int k = 0; k < 10; ++k) { const float d = xv[k] - mean; ss = fmaf(d, d, ss); }
      const float var = wave_sum(ss) * (1.0f / DM);
      const float mb = rbf(mean);
      const float sd = rbf(sqrtf(rbf(rbf(var) + 1e-5f)));
      const unsigned n0 = tobf(rbf(bf1(xo0) - mb) / sd);
      const unsigned n1 = tobf(rbf(bf1(xo1) - mb) / sd);
      const unsigned n2 = tobf(rbf(bf1(xo2) - mb) / sd);
      const unsigned d0 = n0 | (n0 << 16), d1 = n1 | (n1 << 16), d2 = n2 | (n2 << 16);
      unsigned* xq = &lds[L_XQ + w * QROWS + (lane & 3) * 40 + (lane >> 2)];
      unsigned* x1 = &lds[L_X1 + w * QROWS + (lane & 7) * 20 + (lane >> 3)];
      xq[0] = d0; xq[16] = d1;
      x1[0] = d0; x1[8] = d1;
      if (lane < 32) { xq[32] = d2; x1[16] = d2; }
    }
    __builtin_amdgcn_fence(__ATOMIC_RELEASE, "wavefront");
    __builtin_amdgcn_wave_barrier();
    __builtin_amdgcn_fence(__ATOMIC_ACQUIRE, "wavefront");
    {
      f32x4 a0 = {0.f, 0.f, 0.f, 0.f}, a1 = {0.f, 0.f, 0.f, 0.f};
#pragma unroll
      for (int i4 = 0; i4 < 10; ++i4) {
        const u32x4 xd = *reinterpret_cast<const u32x4*>(&lds[L_XQ + w * QROWS + r * 40 + i4 * 4]);
        a0 = gemv_mfma(wr[4 * i4 + 0], xd[0], msk8, a0);
        a1 = gemv_mfma(wr[4 * i4 + 1], xd[1], msk8, a1);
        a0 = gemv_mfma(wr[4 * i4 + 2], xd[2], msk8, a0);
        a1 = gemv_mfma(wr[4 * i4 + 3], xd[3], msk8, a1);
      }
      a0 += a1;
      if (c < 8) {
#pragma unroll
        for (int g = 0; g < 4; ++g) ldsf[L_QP + w * DH + 8 * (4 * r + g) + c] = a0[g];
      }
    }
    __builtin_amdgcn_s_setprio(0);
    STAMP(1);
    __syncthreads();
    STAMP(2);
    {
      const unsigned ch = min((unsigned)chunk0 + c8, (unsigned)(FF / 8 - 1));
      const char* p = w1 + (size_t)(QROWS * w) * (FF * 2);
      const unsigned lo = r8 * (FF * 2) + ch * 16;
#pragma unroll
      for (int i = 0; i < 20; ++i) { wr[i] = ld_w(p + (size_t)(8 * i) * (FF * 2) + lo); PACE }
      SB
      const char* p2 = w2 + (size_t)(FS * js) * (DM * 2);
#pragma unroll
      for (int g = 0; g < 4; ++g) {
        const unsigned lo2 = min(16u * w + 4 * g + r, (unsigned)(FS - 1)) * (DM * 2) + c * 16;
#pragma unroll
        for (int s = 0; s < 5; ++s) { wr[20 + 5 * g + s] = ld_w(p2 + s * 256 + lo2); PACE }
      }
      SB
      f32x4 a0 = {0.f, 0.f, 0.f, 0.f}, a1 = {0.f, 0.f, 0.f, 0.f};
#pragma unroll
      for (int i4 = 0; i4 < 5; ++i4) {
        const u32x4 xd = *reinterpret_cast<const u32x4*>(&lds[L_X1 + w * QROWS + r8 * 20 + i4 * 4]);
        a0 = gemv_mfma(wr[4 * i4 + 0], xd[0], msk16, a0);
        a1 = gemv_mfma(wr[4 * i4 + 1], xd[1], msk16, a1);
        a0 = gemv_mfma(wr[4 * i4 + 2], xd[2], msk16, a0);
        a1 = gemv_mfma(wr[4 * i4 + 3], xd[3], msk16, a1);
      }
      a0 += a1;
      if ((r >> 1) == (c >> 3)) {
#pragma unroll
        for (int g = 0; g < 4; ++g) ldsf[L_HP + w * 128 + (r >> 1) * 64 + 8 * (4 * (r & 1) + g) + c8] = a0[g];
      }
      __builtin_amdgcn_fence(__ATOMIC_RELEASE, "workgroup", "local");
      if (lane == 0) atomicAdd(&lds[L_CNT + 1], 1u);
      while (__hip_atomic_load(&lds[L_CNT + 1], __ATOMIC_RELAXED, __HIP_MEMORY_SCOPE_WORKGROUP) < (unsigned)NHLP) __builtin_amdgcn_s_sleep(2);
      __builtin_amdgcn_fence(__ATOMIC_ACQUIRE, "workgroup", "local");
      unsigned hd = 0u;
      {
        const unsigned off = FS * js - chunk0 * 8;
        float sm = 0.f;
#pragma unroll
        for (int ww = 0; ww < 2 * NHLP; ++ww) sm += ldsf[L_HP + ww * 64 + ((off + lane) & 63)];
        const float hb = rbf(rbf(sm) + bf1(b1u));
        const float u2 = 1.5957691216057308f * (hb + 0.044715f * hb * hb * hb);
        const unsigned hv = tobf(hb / (1.0f + __expf(-u2)));
        hd = (lane < (unsigned)FS) ? (hv | (hv << 16)) : 0u;
      }
      unsigned hg[4];
#pragma unroll
      for (int g = 0; g < 4; ++g) hg[g] = __builtin_amdgcn_ds_bpermute((int)((16 * w + 4 * g + r) * 4), (int)hd);
#pragma unroll
      for (int s = 0; s < 5; ++s) {
        f32x4 mm = {0.f, 0.f, 0.f, 0.f};
#pragma unroll
        for (int g = 0; g < 4; ++g) mm = gemv_mfma(wr[20 + 5 * g + s], hg[g], msk8, mm);
        if (c < 8) *reinterpret_cast<f32x4*>(&ldsf[L_PP + ((w * 10 + 5 + s) * 32 + r * 8 + c) * 4]) = mm;
      }
    }
    SB
    {
      const char* wop = wo + (size_t)(h * DH + 32 * w) * (DM * 2);
      const unsigned wlo = r * (DM * 2) + c * 16;
#pragma unroll
      for (int u = 0; u < 40; ++u) { wr[u] = ld_w(wop + (u / 5) * (4 * DM * 2) + (u % 5) * 256 + wlo); PACE }
    }
    SB
    STAMP(3);
    __syncthreads();
    STAMP(5);
    unsigned od[8];
    {
      float mv[NSTR], lv[NSTR], av[NSTR];
#pragma unroll
      for (int ww = 0; ww < NSTR; ++ww) {
        const float* rec = &ldsf[L_MG + ww * MG_REC];
        mv[ww] = rec[0]; lv[ww] = rec[1]; av[ww] = rec[4 + 32 * w + (lane & 31)];
      }
      float M = mv[0];
#pragma unroll
      for (int ww = 1; ww < NSTR; ++ww) M = fmaxf(M, mv[ww]);
      float L = 0.f, o = 0.f;
#pragma unroll
      for (int ww = 0; ww < NSTR; ++ww) {
        const float wt = __builtin_amdgcn_exp2f(mv[ww] - M);
        L = fmaf(wt, lv[ww], L);
        o = fmaf(wt, av[ww], o);
      }
      const unsigned ob = tobf(o / L);
      const unsigned odup = ob | (ob << 16);
#pragma unroll
      for (int g = 0; g < 8; ++g) od[g] = __builtin_amdgcn_ds_bpermute((int)((4 * g + r) * 4), (int)odup);
    }
    {
      f32x4 a[5];
#pragma unroll
      for (int s = 0; s < 5; ++s) a[s] = f32x4{0.f, 0.f, 0.f, 0.f};
#pragma unroll
      for (int g = 0; g < 8; ++g) {
#pragma unroll
        for (int s = 0; s < 5; ++s) a[s] = gemv_mfma(wr[5 * g + s], od[g], msk8, a[s]);
      }
      if (c < 8) {
#pragma unroll
        for (int s = 0; s < 5; ++s) *reinterpret_cast<f32x4*>(&ldsf[L_PP + ((w * 10 + s) * 32 + r * 8 + c) * 4]) = a[s];
      }
    }
    __syncthreads();
    STAMP(6);
  }
  if (tid < 320) {
    f32x4 s = {0.f, 0.f, 0.f, 0.f};
#pragma unroll
    for (int ww = 0; ww < NHLP; ++ww) s += *reinterpret_cast<const f32x4*>(&ldsf[L_PP + (ww * 320 + tid) * 4]);
    *reinterpret_cast<f32x4*>(spart + (size_t)bid * 1280 + tid * 4) = s;
  }
  STAMP(7);
  STAMP_FLUSH(0);
}

__global__ void __launch_bounds__(256) final_kernel(const unsigned short* __restrict__ x, const unsigned short* __restrict__ b2,
                                                    const float* __restrict__ spart, unsigned short* __restrict__ out) {
  __shared__ __attribute__((aligned(16))) float red[2][16][64];
  const int b = blockIdx.x / 10, seg = blockIdx.x % 10;
  STAMP_DECL
  STAMP(0);
  const int tid = threadIdx.x, cq = tid & 15, g = tid >> 4;
  const size_t base = (size_t)(b * NH + 2 * g) * 1280 + seg * 64 + cq * 4;
  const f32x4 s0 = *reinterpret_cast<const f32x4*>(spart + base);
  const f32x4 s1 = *reinterpret_cast<const f32x4*>(spart + base + 1280);
  const f32x4 m0 = *reinterpret_cast<const f32x4*>(spart + base + 640);
  const f32x4 m1 = *reinterpret_cast<const f32x4*>(spart + base + 1280 + 640);
  const int p = seg * 64 + (tid & 63);
  const int n = (p >> 7) * 128 + 8 * (4 * ((p >> 5) & 3) + (p & 3)) + ((p >> 2) & 7);
  float b2v = bf1(b2[n]);
  float xv = bf1(x[b * DM + n]);
  asm volatile("" : "+v"(b2v), "+v"(xv));
  *reinterpret_cast<f32x4*>(&red[0][g][cq * 4]) = s0 + s1;
  *reinterpret_cast<f32x4*>(&red[1][g][cq * 4]) = m0 + m1;
  __syncthreads();
  if (tid < 64) {
    float s = 0.f, mm = 0.f;
#pragma unroll
    for (int k = 0; k < 16; ++k) { s += red[0][k][tid]; mm += red[1][k][tid]; }
    const float sb = rbf(s);
    const float mb = rbf(rbf(mm) + b2v);
    const float gt = rbf(mb / (1.0f + __expf(-mb)));
    const float res = xv + rbf(sb * gt);
    out[b * DM + n] = tobf(res);
  }
  STAMP(1);
  STAMP_FLUSH(256);
}

}

extern "C" void kernel_launch(void* const* d_in, const int* in_sizes, int n_in, void* d_out, int out_size, void* d_ws,
                              size_t ws_size, hipStream_t stream) {
  const unsigned short* x = (const unsigned short*)d_in[0];
  const char* kc = (const char*)d_in[1];
  const char* vc = (const char*)d_in[2];
  const char* wq = (const char*)d_in[3];
  const char* wo = (const char*)d_in[4];
  const char* w1 = (const char*)d_in[6];
  const unsigned short* b1 = (const unsigned short*)d_in[7];
  const char* w2 = (const char*)d_in[8];
  const unsigned short* b2 = (const unsigned short*)d_in[9];
  float* spart = (float*)d_ws;
  fused_attn_kernel<<<NB * NH, NT, 0, stream>>>(x, kc, vc, wq, wo, w1, b1, w2, spart);
  final_kernel<<<NB * 10, 256, 0, stream>>>(x, b2, spart, (unsigned short*)d_out);
}
```

```cpp
#include <hip/hip_runtime.h>
#include <hip/hip_bf16.h>
#include <stdint.h>

#define STAMP_DECL
#define STAMP(k) do { } while (0)
#define STAMP_FLUSH(base) do { } while (0)

#define V_WNT 1
#define V_SLP 16
#define V_DYN 0
#define V_BOOT 0

namespace {

constexpr int NB = 8, DM = 640, NH = 32, SK = 4096, DH = 128, FF = 1728;
constexpr int NW = 8;
constexpr int NT = NW * 64;
constexpr int NHLP = 4, NSTR = 4;
constexpr int FS = FF / NH;
constexpr int QROWS = DM / NHLP;
constexpr int NSC = SK / 64;
constexpr int NSC_DYN = V_BOOT ? NSC - 2 : NSC;

typedef unsigned u32x4 __attribute__((ext_vector_type(4)));
typedef float f32x4 __attribute__((ext_vector_type(4)));
typedef __bf16 bf16x2 __attribute__((ext_vector_type(2)));
typedef __bf16 bf16x8 __attribute__((ext_vector_type(8)));

__device__ __forceinline__ float bf_lo(unsigned u) { return __builtin_bit_cast(float, u << 16); }
__device__ __forceinline__ float bf_hi(unsigned u) { return __builtin_bit_cast(float, u & 0xffff0000u); }
__device__ __forceinline__ float bf1(unsigned short u) { return __builtin_bit_cast(float, (unsigned)u << 16); }
__device__ __forceinline__ float rbf(float x) { return (float)(__bf16)x; }
__device__ __forceinline__ unsigned short tobf(float x) { return __builtin_bit_cast(unsigned short, (__bf16)x); }
__device__ __forceinline__ unsigned pack2(float lo, float hi) { return (unsigned)tobf(lo) | ((unsigned)tobf(hi) << 16); }

__device__ __forceinline__ float dot2(unsigned k, unsigned q, float acc) {
  return __builtin_amdgcn_fdot2_f32_bf16(__builtin_bit_cast(bf16x2, k), __builtin_bit_cast(bf16x2, q), acc, false);
}
template <int CTRL> __device__ __forceinline__ float dpp(float x) {
  return __builtin_bit_cast(float, __builtin_amdgcn_mov_dpp(__builtin_bit_cast(int, x), CTRL, 0xf, 0xf, true));
}
__device__ __forceinline__ float row16_sum(float x) {
  x += dpp<0xB1>(x);
  x += dpp<0x4E>(x);
  x += dpp<0x141>(x);
  x += dpp<0x140>(x);
  return x;
}
__device__ __forceinline__ float xrow_sum(float x) {
  auto s = __builtin_amdgcn_permlane16_swap(__float_as_uint(x), __float_as_uint(x), false, false);
  x = __uint_as_float(s[0]) + __uint_as_float(s[1]);
  auto t = __builtin_amdgcn_permlane32_swap(__float_as_uint(x), __float_as_uint(x), false, false);
  return __uint_as_float(t[0]) + __uint_as_float(t[1]);
}
__device__ __forceinline__ float xrow_max(float x) {
  auto s = __builtin_amdgcn_permlane16_swap(__float_as_uint(x), __float_as_uint(x), false, false);
  x = fmaxf(__uint_as_float(s[0]), __uint_as_float(s[1]));
  auto t = __builtin_amdgcn_permlane32_swap(__float_as_uint(x), __float_as_uint(x), false, false);
  return fmaxf(__uint_as_float(t[0]), __uint_as_float(t[1]));
}
__device__ __forceinline__ float wave_sum(float x) { return xrow_sum(row16_sum(x)); }

__device__ __forceinline__ u32x4 ld_nt(const char* p) { return __builtin_nontemporal_load(reinterpret_cast<const u32x4*>(p)); }
__device__ __forceinline__ u32x4 ld_w(const char* p) { return __builtin_nontemporal_load(reinterpret_cast<const u32x4*>(p)); }

__device__ __forceinline__ f32x4 gemv_mfma(u32x4 a, unsigned xdup, u32x4 msk, f32x4 c) {
  const u32x4 bsel = {xdup & msk[0], xdup & msk[1], xdup & msk[2], xdup & msk[3]};
  return __builtin_amdgcn_mfma_f32_16x16x32_bf16(__builtin_bit_cast(bf16x8, a), __builtin_bit_cast(bf16x8, bsel), c, 0, 0, 0);
}

constexpr int L_XQ = 0;
constexpr int L_X1 = L_XQ + DM;
constexpr int L_QP = L_X1 + DM;
constexpr int L_HP = L_QP + NHLP * DH;
constexpr int L_HA = L_HP + NHLP * 128;
constexpr int L_MG = L_HA + 64;
constexpr int MG_REC = DH + 4;
constexpr int L_PP = L_MG + NSTR * MG_REC;
constexpr int L_CNT = L_PP + NHLP * 10 * 128;
constexpr int L_END = L_CNT + 4;

struct Slot { u32x4 k[4]; u32x4 v[4]; };

__global__ void __launch_bounds__(NT, 2) fused_attn_kernel(
    const unsigned short* __restrict__ x, const char* __restrict__ kc, const char* __restrict__ vc,
    const char* __restrict__ wq, const char* __restrict__ wo, const char* __restrict__ w1,
    const unsigned short* __restrict__ b1, const char* __restrict__ w2, float* __restrict__ spart) {
  __shared__ __attribute__((aligned(16))) unsigned lds[L_END];
  float* ldsf = reinterpret_cast<float*>(lds);
  const int tid = threadIdx.x;
  const unsigned lane = tid & 63;
  const int w = __builtin_amdgcn_readfirstlane(tid >> 6);
  const unsigned r = lane >> 4, c = lane & 15;
  const int bid = blockIdx.x;
  const int h = bid & 31, b = bid >> 5;
  STAMP_DECL
  STAMP(0);
#define SB __builtin_amdgcn_sched_barrier(0);
#define PACE SB __builtin_amdgcn_s_sleep(V_SLP); SB

  if (w >= NHLP) {
    const int j = w - NHLP;
    const char* kb = kc + (size_t)bid * (SK * DH * 2);
    const char* vb = vc + (size_t)bid * (SK * DH * 2);
    const unsigned lo16 = lane * 16;
#define SC_OFF(d, sc) ((size_t)((sc) >> 2) * 65536 + ((sc) & 3) * 4096 + (d) * 16384)
    Slot S[4];
#define LOAD_K(Sd, d, sc) _Pragma("unroll") for (int t = 0; t < 4; ++t) Sd.k[t] = ld_nt(kb + SC_OFF(d, sc) + t * 1024 + lo16);
#define LOAD_V(Sd, d, sc) _Pragma("unroll") for (int t = 0; t < 4; ++t) Sd.v[t] = ld_nt(vb + SC_OFF(d, sc) + t * 1024 + lo16);
    LOAD_K(S[0], 0, j) SB LOAD_V(S[0], 0, j) SB LOAD_K(S[1], 1, j) SB LOAD_V(S[1], 1, j) SB
    LOAD_K(S[2], 2, j) SB LOAD_V(S[2], 2, j) SB LOAD_K(S[3], 3, j) SB LOAD_V(S[3], 3, j) SB
    STAMP(1);
    __syncthreads();
    STAMP(2);
    u32x4 q;
    {
      const float* qp = &ldsf[L_QP + r * DH + c * 8];
      f32x4 s0 = *reinterpret_cast<const f32x4*>(qp);
      f32x4 s1 = *reinterpret_cast<const f32x4*>(qp + 4);
#pragma unroll
      for (int k = 0; k < 4; ++k) { s0[k] = xrow_sum(s0[k]); s1[k] = xrow_sum(s1[k]); }
      q[0] = pack2(s0[0], s0[1]); q[1] = pack2(s0[2], s0[3]); q[2] = pack2(s1[0], s1[1]); q[3] = pack2(s1[2], s1[3]);
    }
    asm volatile("" : "+v"(q));
    STAMP(3);

    constexpr float C2 = 0.08838834764831845f * 1.4426950408889634f;
    float m = -INFINITY, l = 0.f;
    float acc[8];
#pragma unroll
    for (int k = 0; k < 8; ++k) acc[k] = 0.f;

#define QK_PART(Sd)                                                   \
  float p_[4];                                                        \
  {                                                                   \
    float s_[4];                                                      \
    _Pragma("unroll") for (int t = 0; t < 4; ++t) {                   \
      float dd = 0.f;                                                 \
      _Pragma("unroll") for (int k = 0; k < 4; ++k) dd = dot2(Sd.k[t][k], q[k], dd); \
      s_[t] = dd;                                                     \
    }                                                                 \
    _Pragma("unroll") for (int t = 0; t < 4; ++t) s_[t] = row16_sum(s_[t]) * C2; \
    const float mn = fmaxf(fmaxf(m, fmaxf(s_[0], s_[1])), fmaxf(s_[2], s_[3])); \
    const float alpha = __builtin_amdgcn_exp2f(m - mn);               \
    m = mn;                                                           \
    _Pragma("unroll") for (int t = 0; t < 4; ++t) p_[t] = __builtin_amdgcn_exp2f(s_[t] - mn); \
    l = fmaf(l, alpha, (p_[0] + p_[1]) + (p_[2] + p_[3]));            \
    _Pragma("unroll") for (int k = 0; k < 8; ++k) acc[k] *= alpha;    \
  }
#define PV_PART(Sd)                                                   \
  _Pragma("unroll") for (int t = 0; t < 4; ++t) {                     \
    _Pragma("unroll") for (int k = 0; k < 4; ++k) {                   \
      acc[2 * k] = fmaf(p_[t], bf_lo(Sd.v[t][k]), acc[2 * k]);        \
      acc[2 * k + 1] = fmaf(p_[t], bf_hi(Sd.v[t][k]), acc[2 * k + 1]); \
    }                                                                 \
  }                                                                   \
  asm volatile("" : "+v"(acc[0]), "+v"(acc[1]), "+v"(acc[2]), "+v"(acc[3]), "+v"(acc[4]), "+v"(acc[5]), "+v"(acc[6]), "+v"(acc[7]));
#define STEP(d) { QK_PART(S[d]) LOAD_K(S[d], d, nxt) SB PV_PART(S[d]) LOAD_V(S[d], d, nxt) SB }
#define STEP_LAST(Sd) { QK_PART(Sd) SB PV_PART(Sd) SB }

    int nxt = NSTR + j;
    while (nxt < NSC_DYN) {
      STEP(0) STEP(1) STEP(2) STEP(3)
      nxt += NSTR;
    }
    STEP_LAST(S[0]) STEP_LAST(S[1]) STEP_LAST(S[2]) STEP_LAST(S[3])
    STAMP(4);
    {
      const float mw = xrow_max(m);
      const float sc = __builtin_amdgcn_exp2f(m - mw);
      const float lw = xrow_sum(l * sc);
#pragma unroll
      for (int k = 0; k < 8; ++k) acc[k] = xrow_sum(acc[k] * sc);
      float* rec = &ldsf[L_MG + j * MG_REC];
      if (r == 0) {
        *reinterpret_cast<f32x4*>(rec + 4 + c * 8) = f32x4{acc[0], acc[1], acc[2], acc[3]};
        *reinterpret_cast<f32x4*>(rec + 4 + c * 8 + 4) = f32x4{acc[4], acc[5], acc[6], acc[7]};
        if (c == 0) { rec[0] = mw; rec[1] = lw; }
      }
    }
    __syncthreads();
    STAMP(5);
    __syncthreads();
    STAMP(6);
  } else {
    __builtin_amdgcn_s_setprio(3);
    const int js = (h & 7) * 4 + (h >> 3);
    const unsigned r8 = lane >> 3, c8 = lane & 7;
    const int chunk0 = (FS * js) >> 3;
    const unsigned short* xb = x + b * DM;
    const u32x4 xa = *reinterpret_cast<const u32x4*>(xb + lane * 8);
    const unsigned xt = *reinterpret_cast<const unsigned*>(xb + 512 + lane * 2);
    const unsigned short xo0 = xb[QROWS * w + lane];
    const unsigned short xo1 = xb[QROWS * w + 64 + lane];
    const unsigned short xo2 = xb[QROWS * w + 128 + (lane & 31)];
    const unsigned short b1u = b1[FS * js + min(lane, (unsigned)(FS - 1))];
    SB
    u32x4 wr[40];
    {
      const char* p = wq + (size_t)(QROWS * w) * (NH * DH * 2) + h * (DH * 2);
      const unsigned lo = r * (NH * DH * 2) + c * 16;
#pragma unroll
      for (int i = 0; i < 40; ++i) {
        wr[i] = ld_w(p + (size_t)(4 * i) * (NH * DH * 2) + lo);
        if ((i & 3) == 0) { SB }
      }
    }
    SB
    if (tid == 0) { lds[L_CNT] = 2 * NSTR; lds[L_CNT + 1] = 0; lds[L_CNT + 2] = 0; }
    u32x4 msk8, msk16;
#pragma unroll
    for (int k = 0; k < 4; ++k) {
      const unsigned m16 = (c8 == 2u * k) ? 0x0000ffffu : ((c8 == 2u * k + 1) ? 0xffff0000u : 0u);
      msk16[k] = m16;
      msk8[k] = (c < 8) ? m16 : 0u;
    }
    {
      float xv[10];
#pragma unroll
      for (int k = 0; k < 4; ++k) { xv[2 * k] = bf_lo(xa[k]); xv[2 * k + 1] = bf_hi(xa[k]); }
      xv[8] = bf_lo(xt); xv[9] = bf_hi(xt);
      float sm = 0.f;
#pragma unroll
      for (int k = 0; k < 10; ++k) sm += xv[k];
      const float mean = wave_sum(sm) * (1.0f / DM);
      float ss = 0.f;
#pragma unroll
      for (int k = 0; k < 10; ++k) { const float d = xv[k] - mean; ss = fmaf(d, d, ss); }
      const float var = wave_sum(ss) * (1.0f / DM);
      const float mb = rbf(mean);
      const float sd = rbf(sqrtf(rbf(rbf(var) + 1e-5f)));
      const unsigned n0 = tobf(rbf(bf1(xo0) - mb) / sd);
      const unsigned n1 = tobf(rbf(bf1(xo1) - mb) / sd);
      const unsigned n2 = tobf(rbf(bf1(xo2) - mb) / sd);
      const unsigned d0 = n0 | (n0 << 16), d1 = n1 | (n1 << 16), d2 = n2 | (n2 << 16);
      unsigned* xq = &lds[L_XQ + w * QROWS + (lane & 3) * 40 + (lane >> 2)];
      unsigned* x1 = &lds[L_X1 + w * QROWS + (lane & 7) * 20 + (lane >> 3)];
      xq[0] = d0; xq[16] = d1;
      x1[0] = d0; x1[8] = d1;
      if (lane < 32) { xq[32] = d2; x1[16] = d2; }
    }
    __builtin_amdgcn_fence(__ATOMIC_RELEASE, "wavefront");
    __builtin_amdgcn_wave_barrier();
    __builtin_amdgcn_fence(__ATOMIC_ACQUIRE, "wavefront");
    {
      f32x4 a0 = {0.f, 0.f, 0.f, 0.f}, a1 = {0.f, 0.f, 0.f, 0.f};
#pragma unroll
      for (int i4 = 0; i4 < 10; ++i4) {
        const u32x4 xd = *reinterpret_cast<const u32x4*>(&lds[L_XQ + w * QROWS + r * 40 + i4 * 4]);
        a0 = gemv_mfma(wr[4 * i4 + 0], xd[0], msk8, a0);
        a1 = gemv_mfma(wr[4 * i4 + 1], xd[1], msk8, a1);
        a0 = gemv_mfma(wr[4 * i4 + 2], xd[2], msk8, a0);
        a1 = gemv_mfma(wr[4 * i4 + 3], xd[3], msk8, a1);
      }
      a0 += a1;
      if (c < 8) {
#pragma unroll
        for (int g = 0; g < 4; ++g) ldsf[L_QP + w * DH + 8 * (4 * r + g) + c] = a0[g];
      }
    }
    __builtin_amdgcn_s_setprio(0);
    STAMP(1);
    __syncthreads();
    STAMP(2);
    {
      const unsigned ch = min((unsigned)chunk0 + c8, (unsigned)(FF / 8 - 1));
      const char* p = w1 + (size_t)(QROWS * w) * (FF * 2);
      const unsigned lo = r8 * (FF * 2) + ch * 16;
#pragma unroll
      for (int i = 0; i < 20; ++i) { wr[i] = ld_w(p + (size_t)(8 * i) * (FF * 2) + lo); PACE }
      SB
      const char* p2 = w2 + (size_t)(FS * js) * (DM * 2);
#pragma unroll
      for (int g = 0; g < 4; ++g) {
        const unsigned lo2 = min(16u * w + 4 * g + r, (unsigned)(FS - 1)) * (DM * 2) + c * 16;
#pragma unroll
        for (int s = 0; s < 5; ++s) { wr[20 + 5 * g + s] = ld_w(p2 + s * 256 + lo2); PACE }
      }
      SB
      f32x4 a0 = {0.f, 0.f, 0.f, 0.f}, a1 = {0.f, 0.f, 0.f, 0.f};
#pragma unroll
      for (int i4 = 0; i4 < 5; ++i4) {
        const u32x4 xd = *reinterpret_cast<const u32x4*>(&lds[L_X1 + w * QROWS + r8 * 20 + i4 * 4]);
        a0 = gemv_mfma(wr[4 * i4 + 0], xd[0], msk16, a0);
        a1 = gemv_mfma(wr[4 * i4 + 1], xd[1], msk16, a1);
        a0 = gemv_mfma(wr[4 * i4 + 2], xd[2], msk16, a0);
        a1 = gemv_mfma(wr[4 * i4 + 3], xd[3], msk16, a1);
      }
      a0 += a1;
      if ((r >> 1) == (c >> 3)) {
#pragma unroll
        for (int g = 0; g < 4; ++g) ldsf[L_HP + w * 128 + (r >> 1) * 64 + 8 * (4 * (r & 1) + g) + c8] = a0[g];
      }
      __builtin_amdgcn_fence(__ATOMIC_RELEASE, "workgroup", "local");
      if (lane == 0) atomicAdd(&lds[L_CNT + 1], 1u);
      while (__hip_atomic_load(&lds[L_CNT + 1], __ATOMIC_RELAXED, __HIP_MEMORY_SCOPE_WORKGROUP) < (unsigned)NHLP) __builtin_amdgcn_s_sleep(2);
      __builtin_amdgcn_fence(__ATOMIC_ACQUIRE, "workgroup", "local");
      unsigned hd = 0u;
      {
        const unsigned off = FS * js - chunk0 * 8;
        float sm = 0.f;
#pragma unroll
        for (int ww = 0; ww < 2 * NHLP; ++ww) sm += ldsf[L_HP + ww * 64 + ((off + lane) & 63)];
        const float hb = rbf(rbf(sm) + bf1(b1u));
        const float u2 = 1.5957691216057308f * (hb + 0.044715f * hb * hb * hb);
        const unsigned hv = tobf(hb / (1.0f + __expf(-u2)));
        hd = (lane < (unsigned)FS) ? (hv | (hv << 16)) : 0u;
      }
      unsigned hg[4];
#pragma unroll
      for (int g = 0; g < 4; ++g) hg[g] = __builtin_amdgcn_ds_bpermute((int)((16 * w + 4 * g + r) * 4), (int)hd);
#pragma unroll
      for (int s = 0; s < 5; ++s) {
        f32x4 mm = {0.f, 0.f, 0.f, 0.f};
#pragma unroll
        for (int g = 0; g < 4; ++g) mm = gemv_mfma(wr[20 + 5 * g + s], hg[g], msk8, mm);
        if (c < 8) *reinterpret_cast<f32x4*>(&ldsf[L_PP + ((w * 10 + 5 + s) * 32 + r * 8 + c) * 4]) = mm;
      }
    }
    SB
    {
      const char* wop = wo + (size_t)(h * DH + 32 * w) * (DM * 2);
      const unsigned wlo = r * (DM * 2) + c * 16;
#pragma unroll
      for (int u = 0; u < 40; ++u) { wr[u] = ld_w(wop + (u / 5) * (4 * DM * 2) + (u % 5) * 256 + wlo); PACE }
    }
    SB
    STAMP(3);
    __syncthreads();
    STAMP(5);
    unsigned od[8];
    {
      float mv[NSTR], lv[NSTR], av[NSTR];
#pragma unroll
      for (int ww = 0; ww < NSTR; ++ww) {
        const float* rec = &ldsf[L_MG + ww * MG_REC];
        mv[ww] = rec[0]; lv[ww] = rec[1]; av[ww] = rec[4 + 32 * w + (lane & 31)];
      }
      float M = mv[0];
#pragma unroll
      for (int ww = 1; ww < NSTR; ++ww) M = fmaxf(M, mv[ww]);
      float L = 0.f, o = 0.f;
#pragma unroll
      for (int ww = 0; ww < NSTR; ++ww) {
        const float wt = __builtin_amdgcn_exp2f(mv[ww] - M);
        L = fmaf(wt, lv[ww], L);
        o = fmaf(wt, av[ww], o);
      }
      const unsigned ob = tobf(o / L);
      const unsigned odup = ob | (ob << 16);
#pragma unroll
      for (int g = 0; g < 8; ++g) od[g] = __builtin_amdgcn_ds_bpermute((int)((4 * g + r) * 4), (int)odup);
    }
    {
      f32x4 a[5];
#pragma unroll
      for (int s = 0; s < 5; ++s) a[s] = f32x4{0.f, 0.f, 0.f, 0.f};
#pragma unroll
      for (int g = 0; g < 8; ++g) {
#pragma unroll
        for (int s = 0; s < 5; ++s) a[s] = gemv_mfma(wr[5 * g + s], od[g], msk8, a[s]);
      }
      if (c < 8) {
#pragma unroll
        for (int s = 0; s < 5; ++s) *reinterpret_cast<f32x4*>(&ldsf[L_PP + ((w * 10 + s) * 32 + r * 8 + c) * 4]) = a[s];
      }
    }
    __syncthreads();
    STAMP(6);
  }
  if (tid < 320) {
    f32x4 s = {0.f, 0.f, 0.f, 0.f};
#pragma unroll
    for (int ww = 0; ww < NHLP; ++ww) s += *reinterpret_cast<const f32x4*>(&ldsf[L_PP + (ww * 320 + tid) * 4]);
    *reinterpret_cast<f32x4*>(spart + (size_t)bid * 1280 + tid * 4) = s;
  }
  STAMP(7);
  STAMP_FLUSH(0);
}

__global__ void __launch_bounds__(256) final_kernel(const unsigned short* __restrict__ x, const unsigned short* __restrict__ b2,
                                                    const float* __restrict__ spart, unsigned short* __restrict__ out) {
  __shared__ __attribute__((aligned(16))) float red[2][16][64];
  const int b = blockIdx.x / 10, seg = blockIdx.x % 10;
  STAMP_DECL
  STAMP(0);
  const int tid = threadIdx.x, cq = tid & 15, g = tid >> 4;
  const size_t base = (size_t)(b * NH + 2 * g) * 1280 + seg * 64 + cq * 4;
  const f32x4 s0 = *reinterpret_cast<const f32x4*>(spart + base);
  const f32x4 s1 = *reinterpret_cast<const f32x4*>(spart + base + 1280);
  const f32x4 m0 = *reinterpret_cast<const f32x4*>(spart + base + 640);
  const f32x4 m1 = *reinterpret_cast<const f32x4*>(spart + base + 1280 + 640);
  const int p = seg * 64 + (tid & 63);
  const int n = (p >> 7) * 128 + 8 * (4 * ((p >> 5) & 3) + (p & 3)) + ((p >> 2) & 7);
  float b2v = bf1(b2[n]);
  float xv = bf1(x[b * DM + n]);
  asm volatile("" : "+v"(b2v), "+v"(xv));
  *reinterpret_cast<f32x4*>(&red[0][g][cq * 4]) = s0 + s1;
  *reinterpret_cast<f32x4*>(&red[1][g][cq * 4]) = m0 + m1;
  __syncthreads();
  if (tid < 64) {
    float s = 0.f, mm = 0.f;
#pragma unroll
    for (int k = 0; k < 16; ++k) { s += red[0][k][tid]; mm += red[1][k][tid]; }
    const float sb = rbf(s);
    const float mb = rbf(rbf(mm) + b2v);
    const float gt = rbf(mb / (1.0f + __expf(-mb)));
    const float res = xv + rbf(sb * gt);
    out[b * DM + n] = tobf(res);
  }
  STAMP(1);
  STAMP_FLUSH(256);
}

}

extern "C" void kernel_launch(void* const* d_in, const int* in_sizes, int n_in, void* d_out, int out_size, void* d_ws,
                              size_t ws_size, hipStream_t stream) {
  const unsigned short* x = (const unsigned short*)d_in[0];
  const char* kc = (const char*)d_in[1];
  const char* vc = (const char*)d_in[2];
  const char* wq = (const char*)d_in[3];
  const char* wo = (const char*)d_in[4];
  const char* w1 = (const char*)d_in[6];
  const unsigned short* b1 = (const unsigned short*)d_in[7];
  const char* w2 = (const char*)d_in[8];
  const unsigned short* b2 = (const unsigned short*)d_in[9];
  float* spart = (float*)d_ws;
  fused_attn_kernel<<<NB * NH, NT, 0, stream>>>(x, kc, vc, wq, wo, w1, b1, w2, spart);
  final_kernel<<<NB * 10, 256, 0, stream>>>(x, b2, spart, (unsigned short*)d_out);
}
```

```cpp
#include <hip/hip_runtime.h>
#include <hip/hip_bf16.h>
#include <stdint.h>

#define STAMP_DECL
#define STAMP(k) do { } while (0)
#define STAMP_FLUSH(base) do { } while (0)

#define V_WNT 1
#define V_SLP 16

namespace {

constexpr int NB = 8, DM = 640, NH = 32, SK = 4096, DH = 128, FF = 1728;
constexpr int NW = 8;
constexpr int NT = NW * 64;
constexpr int NHLP = 4, NSTR = 4;
constexpr int FS = FF / NH;
constexpr int QROWS = DM / NHLP;

typedef unsigned u32x4 __attribute__((ext_vector_type(4)));
typedef float f32x4 __attribute__((ext_vector_type(4)));
typedef __bf16 bf16x2 __attribute__((ext_vector_type(2)));
typedef __bf16 bf16x8 __attribute__((ext_vector_type(8)));

__device__ __forceinline__ float bf_lo(unsigned u) { return __builtin_bit_cast(float, u << 16); }
__device__ __forceinline__ float bf_hi(unsigned u) { return __builtin_bit_cast(float, u & 0xffff0000u); }
__device__ __forceinline__ float bf1(unsigned short u) { return __builtin_bit_cast(float, (unsigned)u << 16); }
__device__ __forceinline__ float rbf(float x) { return (float)(__bf16)x; }
__device__ __forceinline__ unsigned short tobf(float x) { return __builtin_bit_cast(unsigned short, (__bf16)x); }
__device__ __forceinline__ unsigned pack2(float lo, float hi) { return (unsigned)tobf(lo) | ((unsigned)tobf(hi) << 16); }

__device__ __forceinline__ float dot2(unsigned k, unsigned q, float acc) {
  return __builtin_amdgcn_fdot2_f32_bf16(__builtin_bit_cast(bf16x2, k), __builtin_bit_cast(bf16x2, q), acc, false);
}
template <int CTRL> __device__ __forceinline__ float dpp(float x) {
  return __builtin_bit_cast(float, __builtin_amdgcn_mov_dpp(__builtin_bit_cast(int, x), CTRL, 0xf, 0xf, true));
}
__device__ __forceinline__ float row16_sum(float x) {
  x += dpp<0xB1>(x);
  x += dpp<0x4E>(x);
  x += dpp<0x141>(x);
  x += dpp<0x140>(x);
  return x;
}
__device__ __forceinline__ float xrow_sum(float x) {
  auto s = __builtin_amdgcn_permlane16_swap(__float_as_uint(x), __float_as_uint(x), false, false);
  x = __uint_as_float(s[0]) + __uint_as_float(s[1]);
  auto t = __builtin_amdgcn_permlane32_swap(__float_as_uint(x), __float_as_uint(x), false, false);
  return __uint_as_float(t[0]) + __uint_as_float(t[1]);
}
__device__ __forceinline__ float xrow_max(float x) {
  auto s = __builtin_amdgcn_permlane16_swap(__float_as_uint(x), __float_as_uint(x), false, false);
  x = fmaxf(__uint_as_float(s[0]), __uint_as_float(s[1]));
  auto t = __builtin_amdgcn_permlane32_swap(__float_as_uint(x), __float_as_uint(x), false, false);
  return fmaxf(__uint_as_float(t[0]), __uint_as_float(t[1]));
}
__device__ __forceinline__ float wave_sum(float x) { return xrow_sum(row16_sum(x)); }

__device__ __forceinline__ u32x4 ld_nt(const char* p) { return __builtin_nontemporal_load(reinterpret_cast<const u32x4*>(p)); }
__device__ __forceinline__ u32x4 ld_w(const char* p) { return __builtin_nontemporal_load(reinterpret_cast<const u32x4*>(p)); }

__device__ __forceinline__ f32x4 gemv_mfma(u32x4 a, unsigned xdup, u32x4 msk, f32x4 c) {
  const u32x4 bsel = {xdup & msk[0], xdup & msk[1], xdup & msk[2], xdup & msk[3]};
  return __builtin_amdgcn_mfma_f32_16x16x32_bf16(__builtin_bit_cast(bf16x8, a), __builtin_bit_cast(bf16x8, bsel), c, 0, 0, 0);
}

constexpr int L_XQ = 0;
constexpr int L_X1 = L_XQ + DM;
constexpr int L_QP = L_X1 + DM;
constexpr int L_HP = L_QP + NHLP * DH;
constexpr int L_HA = L_HP + NHLP * 128;
constexpr int L_MG = L_HA + 64;
constexpr int MG_REC = DH + 4;
constexpr int L_PP = L_MG + NSTR * MG_REC;
constexpr int L_SC = L_PP + NHLP * 10 * 128;
constexpr int L_CNT = L_SC + NSTR * 1024;
constexpr int L_END = L_CNT + 4;

__global__ void __launch_bounds__(NT, 2) fused_attn_kernel(
    const unsigned short* __restrict__ x, const char* __restrict__ kc, const char* __restrict__ vc,
    const char* __restrict__ wq, const char* __restrict__ wo, const char* __restrict__ w1,
    const unsigned short* __restrict__ b1, const char* __restrict__ w2, float* __restrict__ spart) {
  __shared__ __attribute__((aligned(16))) unsigned lds[L_END];
  float* ldsf = reinterpret_cast<float*>(lds);
  const int tid = threadIdx.x;
  const unsigned lane = tid & 63;
  const int w = __builtin_amdgcn_readfirstlane(tid >> 6);
  const unsigned r = lane >> 4, c = lane & 15;
  const int bid = blockIdx.x;
  const int h = bid & 31, b = bid >> 5;
  STAMP_DECL
  STAMP(0);
#define SB __builtin_amdgcn_sched_barrier(0);
#define PACE SB __builtin_amdgcn_s_sleep(V_SLP); SB

  if (w >= NHLP) {
    const int j = w - NHLP;
    const char* kb = kc + (size_t)bid * (SK * DH * 2) + j * 4096;
    const char* vb = vc + (size_t)bid * (SK * DH * 2) + j * 4096;
    const unsigned lo16 = lane * 16;
    u32x4 R[8][4];
#define LOAD_P(d, base) _Pragma("unroll") for (int t = 0; t < 4; ++t) R[d][t] = ld_nt((base) + (d) * 16384 + t * 1024 + lo16);
#pragma unroll
    for (int d = 0; d < 8; ++d) { LOAD_P(d, kb) SB }
    STAMP(1);
    __syncthreads();
    STAMP(2);
    u32x4 q;
    {
      const float* qp = &ldsf[L_QP + r * DH + c * 8];
      f32x4 s0 = *reinterpret_cast<const f32x4*>(qp);
      f32x4 s1 = *reinterpret_cast<const f32x4*>(qp + 4);
#pragma unroll
      for (int k = 0; k < 4; ++k) { s0[k] = xrow_sum(s0[k]); s1[k] = xrow_sum(s1[k]); }
      q[0] = pack2(s0[0], s0[1]); q[1] = pack2(s0[2], s0[3]); q[2] = pack2(s1[0], s1[1]); q[3] = pack2(s1[2], s1[3]);
    }
    asm volatile("" : "+v"(q));
    STAMP(3);

    float* scw = &ldsf[L_SC + j * 1024];
    const bool c1 = (c & 1) != 0, c2 = (c & 2) != 0;
    float mx = -INFINITY;
    {
      float* sp = scw + r * 4 + (c & 3);
      const char* nb = kb + 8 * 16384;
      for (int it = 0; it < 8; ++it) {
        if (it == 7) nb = vb;
#pragma unroll
        for (int d = 0; d < 8; ++d) {
          float s_[4];
#pragma unroll
          for (int t = 0; t < 4; ++t) {
            float dd = 0.f;
#pragma unroll
            for (int k = 0; k < 4; ++k) dd = dot2(R[d][t][k], q[k], dd);
            s_[t] = dd;
          }
          asm volatile("" : "+v"(s_[0]), "+v"(s_[1]), "+v"(s_[2]), "+v"(s_[3]));
          LOAD_P(d, nb)
          SB
          const float x0 = (c1 ? s_[1] : s_[0]) + dpp<0xB1>(c1 ? s_[0] : s_[1]);
          const float x1 = (c1 ? s_[3] : s_[2]) + dpp<0xB1>(c1 ? s_[2] : s_[3]);
          float y = (c2 ? x1 : x0) + dpp<0x4E>(c2 ? x0 : x1);
          y += dpp<0x128>(y);
          y += dpp<0x124>(y);
          mx = fmaxf(mx, y);
          if (c < 4) sp[d * 16] = y;
          SB
        }
        sp += 128;
        nb += 8 * 16384;
      }
    }
    constexpr float C2 = 0.08838834764831845f * 1.4426950408889634f;
    float m, l;
    {
      mx = fmaxf(mx, dpp<0xB1>(mx));
      mx = fmaxf(mx, dpp<0x4E>(mx));
      m = xrow_max(mx) * C2;
      __builtin_amdgcn_fence(__ATOMIC_RELEASE, "wavefront");
      __builtin_amdgcn_wave_barrier();
      __builtin_amdgcn_fence(__ATOMIC_ACQUIRE, "wavefront");
      float ls = 0.f;
#pragma unroll
      for (int u = 0; u < 4; ++u) {
        f32x4 v = *reinterpret_cast<const f32x4*>(scw + u * 256 + lane * 4);
#pragma unroll
        for (int k = 0; k < 4; ++k) v[k] = __builtin_amdgcn_exp2f(fmaf(v[k], C2, -m));
        ls += (v[0] + v[1]) + (v[2] + v[3]);
        *reinterpret_cast<f32x4*>(scw + u * 256 + lane * 4) = v;
      }
      l = wave_sum(ls);
      __builtin_amdgcn_fence(__ATOMIC_RELEASE, "wavefront");
      __builtin_amdgcn_wave_barrier();
      __builtin_amdgcn_fence(__ATOMIC_ACQUIRE, "wavefront");
    }
    float acc[8];
#pragma unroll
    for (int k = 0; k < 8; ++k) acc[k] = 0.f;
#define PV_PIECE(d, pp)                                               \
  {                                                                   \
    const f32x4 p_ = *reinterpret_cast<const f32x4*>((pp) + (d) * 16); \
    _Pragma("unroll") for (int t = 0; t < 4; ++t) {                   \
      _Pragma("unroll") for (int k = 0; k < 4; ++k) {                 \
        acc[2 * k] = fmaf(p_[t], bf_lo(R[d][t][k]), acc[2 * k]);      \
        acc[2 * k + 1] = fmaf(p_[t], bf_hi(R[d][t][k]), acc[2 * k + 1]); \
      }                                                               \
    }                                                                 \
    asm volatile("" : "+v"(acc[0]), "+v"(acc[1]), "+v"(acc[2]), "+v"(acc[3]), "+v"(acc[4]), "+v"(acc[5]), "+v"(acc[6]), "+v"(acc[7])); \
  }
    {
      const float* pp = scw + r * 4;
      const char* nb = vb + 8 * 16384;
      for (int it = 0; it < 7; ++it) {
#pragma unroll
        for (int d = 0; d < 8; ++d) { PV_PIECE(d, pp) LOAD_P(d, nb) SB }
        pp += 128;
        nb += 8 * 16384;
      }
#pragma unroll
      for (int d = 0; d < 8; ++d) { PV_PIECE(d, pp) SB }
    }
    STAMP(4);
    {
#pragma unroll
      for (int k = 0; k < 8; ++k) acc[k] = xrow_sum(acc[k]);
      float* rec = &ldsf[L_MG + j * MG_REC];
      if (r == 0) {
        *reinterpret_cast<f32x4*>(rec + 4 + c * 8) = f32x4{acc[0], acc[1], acc[2], acc[3]};
        *reinterpret_cast<f32x4*>(rec + 4 + c * 8 + 4) = f32x4{acc[4], acc[5], acc[6], acc[7]};
        if (c == 0) { rec[0] = m; rec[1] = l; }
      }
    }
    __syncthreads();
    STAMP(5);
    __syncthreads();
    STAMP(6);
  } else {
    __builtin_amdgcn_s_setprio(3);
    const int js = (h & 7) * 4 + (h >> 3);
    const unsigned r8 = lane >> 3, c8 = lane & 7;
    const int chunk0 = (FS * js) >> 3;
    const unsigned short* xb = x + b * DM;
    const u32x4 xa = *reinterpret_cast<const u32x4*>(xb + lane * 8);
    const unsigned xt = *reinterpret_cast<const unsigned*>(xb + 512 + lane * 2);
    const unsigned short xo0 = xb[QROWS * w + lane];
    const unsigned short xo1 = xb[QROWS * w + 64 + lane];
    const unsigned short xo2 = xb[QROWS * w + 128 + (lane & 31)];
    const unsigned short b1u = b1[FS * js + min(lane, (unsigned)(FS - 1))];
    SB
    u32x4 wr[40];
    {
      const char* p = wq + (size_t)(QROWS * w) * (NH * DH * 2) + h * (DH * 2);
      const unsigned lo = r * (NH * DH * 2) + c * 16;
#pragma unroll
      for (int i = 0; i < 40; ++i) {
        wr[i] = ld_w(p + (size_t)(4 * i) * (NH * DH * 2) + lo);
        if ((i & 3) == 0) { SB }
      }
    }
    SB
    if (tid == 0) lds[L_CNT + 1] = 0;
    u32x4 msk8, msk16;
#pragma unroll
    for (int k = 0; k < 4; ++k) {
      const unsigned m16 = (c8 == 2u * k) ? 0x0000ffffu : ((c8 == 2u * k + 1) ? 0xffff0000u : 0u);
      msk16[k] = m16;
      msk8[k] = (c < 8) ? m16 : 0u;
    }
    {
      float xv[10];
#pragma unroll
      for (int k = 0; k < 4; ++k) { xv[2 * k] = bf_lo(xa[k]); xv[2 * k + 1] = bf_hi(xa[k]); }
      xv[8] = bf_lo(xt); xv[9] = bf_hi(xt);
      float sm = 0.f;
#pragma unroll
      for (int k = 0; k < 10; ++k) sm += xv[k];
      const float mean = wave_sum(sm) * (1.0f / DM);
      float ss = 0.f;
#pragma unroll
      for (int k = 0; k < 10; ++k) { const float d = xv[k] - mean; ss = fmaf(d, d, ss); }
      const float var = wave_sum(ss) * (1.0f / DM);
      const float mb = rbf(mean);
      const float sd = rbf(sqrtf(rbf(rbf(var) + 1e-5f)));
      const unsigned n0 = tobf(rbf(bf1(xo0) - mb) / sd);
      const unsigned n1 = tobf(rbf(bf1(xo1) - mb) / sd);
      const unsigned n2 = tobf(rbf(bf1(xo2) - mb) / sd);
      const unsigned d0 = n0 | (n0 << 16), d1 = n1 | (n1 << 16), d2 = n2 | (n2 << 16);
      unsigned* xq = &lds[L_XQ + w * QROWS + (lane & 3) * 40 + (lane >> 2)];
      unsigned* x1 = &lds[L_X1 + w * QROWS + (lane & 7) * 20 + (lane >> 3)];
      xq[0] = d0; xq[16] = d1;
      x1[0] = d0; x1[8] = d1;
      if (lane < 32) { xq[32] = d2; x1[16] = d2; }
    }
    __builtin_amdgcn_fence(__ATOMIC_RELEASE, "wavefront");
    __builtin_amdgcn_wave_barrier();
    __builtin_amdgcn_fence(__ATOMIC_ACQUIRE, "wavefront");
    {
      f32x4 a0 = {0.f, 0.f, 0.f, 0.f}, a1 = {0.f, 0.f, 0.f, 0.f};
#pragma unroll
      for (int i4 = 0; i4 < 10; ++i4) {
        const u32x4 xd = *reinterpret_cast<const u32x4*>(&lds[L_XQ + w * QROWS + r * 40 + i4 * 4]);
        a0 = gemv_mfma(wr[4 * i4 + 0], xd[0], msk8, a0);
        a1 = gemv_mfma(wr[4 * i4 + 1], xd[1], msk8, a1);
        a0 = gemv_mfma(wr[4 * i4 + 2], xd[2], msk8, a0);
        a1 = gemv_mfma(wr[4 * i4 + 3], xd[3], msk8, a1);
      }
      a0 += a1;
      if (c < 8) {
#pragma unroll
        for (int g = 0; g < 4; ++g) ldsf[L_QP + w * DH + 8 * (4 * r + g) + c] = a0[g];
      }
    }
    __builtin_amdgcn_s_setprio(0);
    STAMP(1);
    __syncthreads();
    STAMP(2);
    {
      const unsigned ch = min((unsigned)chunk0 + c8, (unsigned)(FF / 8 - 1));
      const char* p = w1 + (size_t)(QROWS * w) * (FF * 2);
      const unsigned lo = r8 * (FF * 2) + ch * 16;
#pragma unroll
      for (int i = 0; i < 20; ++i) { wr[i] = ld_w(p + (size_t)(8 * i) * (FF * 2) + lo); PACE }
      SB
      const char* p2 = w2 + (size_t)(FS * js) * (DM * 2);
#pragma unroll
      for (int g = 0; g < 4; ++g) {
        const unsigned lo2 = min(16u * w + 4 * g + r, (unsigned)(FS - 1)) * (DM * 2) + c * 16;
#pragma unroll
        for (int s = 0; s < 5; ++s) { wr[20 + 5 * g + s] = ld_w(p2 + s * 256 + lo2); PACE }
      }
      SB
      f32x4 a0 = {0.f, 0.f, 0.f, 0.f}, a1 = {0.f, 0.f, 0.f, 0.f};
#pragma unroll
      for (int i4 = 0; i4 < 5; ++i4) {
        const u32x4 xd = *reinterpret_cast<const u32x4*>(&lds[L_X1 + w * QROWS + r8 * 20 + i4 * 4]);
        a0 = gemv_mfma(wr[4 * i4 + 0], xd[0], msk16, a0);
        a1 = gemv_mfma(wr[4 * i4 + 1], xd[1], msk16, a1);
        a0 = gemv_mfma(wr[4 * i4 + 2], xd[2], msk16, a0);
        a1 = gemv_mfma(wr[4 * i4 + 3], xd[3], msk16, a1);
      }
      a0 += a1;
      if ((r >> 1) == (c >> 3)) {
#pragma unroll
        for (int g = 0; g < 4; ++g) ldsf[L_HP + w * 128 + (r >> 1) * 64 + 8 * (4 * (r & 1) + g) + c8] = a0[g];
      }
      __builtin_amdgcn_fence(__ATOMIC_RELEASE, "workgroup", "local");
      if (lane == 0) atomicAdd(&lds[L_CNT + 1], 1u);
      while (__hip_atomic_load(&lds[L_CNT + 1], __ATOMIC_RELAXED, __HIP_MEMORY_SCOPE_WORKGROUP) < (unsigned)NHLP) __builtin_amdgcn_s_sleep(2);
      __builtin_amdgcn_fence(__ATOMIC_ACQUIRE, "workgroup", "local");
      unsigned hd = 0u;
      {
        const unsigned off = FS * js - chunk0 * 8;
        float sm = 0.f;
#pragma unroll
        for (int ww = 0; ww < 2 * NHLP; ++ww) sm += ldsf[L_HP + ww * 64 + ((off + lane) & 63)];
        const float hb = rbf(rbf(sm) + bf1(b1u));
        const float u2 = 1.5957691216057308f * (hb + 0.044715f * hb * hb * hb);
        const unsigned hv = tobf(hb / (1.0f + __expf(-u2)));
        hd = (lane < (unsigned)FS) ? (hv | (hv << 16)) : 0u;
      }
      unsigned hg[4];
#pragma unroll
      for (int g = 0; g < 4; ++g) hg[g] = __builtin_amdgcn_ds_bpermute((int)((16 * w + 4 * g + r) * 4), (int)hd);
#pragma unroll
      for (int s = 0; s < 5; ++s) {
        f32x4 mm = {0.f, 0.f, 0.f, 0.f};
#pragma unroll
        for (int g = 0; g < 4; ++g) mm = gemv_mfma(wr[20 + 5 * g + s], hg[g], msk8, mm);
        if (c < 8) *reinterpret_cast<f32x4*>(&ldsf[L_PP + ((w * 10 + 5 + s) * 32 + r * 8 + c) * 4]) = mm;
      }
    }
    SB
    {
      const char* wop = wo + (size_t)(h * DH + 32 * w) * (DM * 2);
      const unsigned wlo = r * (DM * 2) + c * 16;
#pragma unroll
      for (int u = 0; u < 40; ++u) { wr[u] = ld_w(wop + (u / 5) * (4 * DM * 2) + (u % 5) * 256 + wlo); PACE }
    }
    SB
    STAMP(3);
    __syncthreads();
    STAMP(5);
    unsigned od[8];
    {
      float mv[NSTR], lv[NSTR], av[NSTR];
#pragma unroll
      for (int ww = 0; ww < NSTR; ++ww) {
        const float* rec = &ldsf[L_MG + ww * MG_REC];
        mv[ww] = rec[0]; lv[ww] = rec[1]; av[ww] = rec[4 + 32 * w + (lane & 31)];
      }
      float M = mv[0];
#pragma unroll
      for (int ww = 1; ww < NSTR; ++ww) M = fmaxf(M, mv[ww]);
      float L = 0.f, o = 0.f;
#pragma unroll
      for (int ww = 0; ww < NSTR; ++ww) {
        const float wt = __builtin_amdgcn_exp2f(mv[ww] - M);
        L = fmaf(wt, lv[ww], L);
        o = fmaf(wt, av[ww], o);
      }
      const unsigned ob = tobf(o / L);
      const unsigned odup = ob | (ob << 16);
#pragma unroll
      for (int g = 0; g < 8; ++g) od[g] = __builtin_amdgcn_ds_bpermute((int)((4 * g + r) * 4), (int)odup);
    }
    {
      f32x4 a[5];
#pragma unroll
      for (int s = 0; s < 5; ++s) a[s] = f32x4{0.f, 0.f, 0.f, 0.f};
#pragma unroll
      for (int g = 0; g < 8; ++g) {
#pragma unroll
        for (int s = 0; s < 5; ++s) a[s] = gemv_mfma(wr[5 * g + s], od[g], msk8, a[s]);
      }
      if (c < 8) {
#pragma unroll
        for (int s = 0; s < 5; ++s) *reinterpret_cast<f32x4*>(&ldsf[L_PP + ((w * 10 + s) * 32 + r * 8 + c) * 4]) = a[s];
      }
    }
    __syncthreads();
    STAMP(6);
  }
  if (tid < 320) {
    f32x4 s = {0.f, 0.f, 0.f, 0.f};
#pragma unroll
    for (int ww = 0; ww < NHLP; ++ww) s += *reinterpret_cast<const f32x4*>(&ldsf[L_PP + (ww * 320 + tid) * 4]);
    *reinterpret_cast<f32x4*>(spart + (size_t)bid * 1280 + tid * 4) = s;
  }
  STAMP(7);
  STAMP_FLUSH(0);
}

__global__ void __launch_bounds__(256) final_kernel(const unsigned short* __restrict__ x, const unsigned short* __restrict__ b2,
                                                    const float* __restrict__ spart, unsigned short* __restrict__ out) {
  __shared__ __attribute__((aligned(16))) float red[2][16][64];
  const int b = blockIdx.x / 10, seg = blockIdx.x % 10;
  STAMP_DECL
  STAMP(0);
  const int tid = threadIdx.x, cq = tid & 15, g = tid >> 4;
  const size_t base = (size_t)(b * NH + 2 * g) * 1280 + seg * 64 + cq * 4;
  const f32x4 s0 = *reinterpret_cast<const f32x4*>(spart + base);
  const f32x4 s1 = *reinterpret_cast<const f32x4*>(spart + base + 1280);
  const f32x4 m0 = *reinterpret_cast<const f32x4*>(spart + base + 640);
  const f32x4 m1 = *reinterpret_cast<const f32x4*>(spart + base + 1280 + 640);
  const int p = seg * 64 + (tid & 63);
  const int n = (p >> 7) * 128 + 8 * (4 * ((p >> 5) & 3) + (p & 3)) + ((p >> 2) & 7);
  float b2v = bf1(b2[n]);
  float xv = bf1(x[b * DM + n]);
  asm volatile("" : "+v"(b2v), "+v"(xv));
  *reinterpret_cast<f32x4*>(&red[0][g][cq * 4]) = s0 + s1;
  *reinterpret_cast<f32x4*>(&red[1][g][cq * 4]) = m0 + m1;
  __syncthreads();
  if (tid < 64) {
    float s = 0.f, mm = 0.f;
#pragma unroll
    for (int k = 0; k < 16; ++k) { s += red[0][k][tid]; mm += red[1][k][tid]; }
    const float sb = rbf(s);
    const float mb = rbf(rbf(mm) + b2v);
    const float gt = rbf(mb / (1.0f + __expf(-mb)));
    const float res = xv + rbf(sb * gt);
    out[b * DM + n] = tobf(res);
  }
  STAMP(1);
  STAMP_FLUSH(256);
}

}

extern "C" void kernel_launch(void* const* d_in, const int* in_sizes, int n_in, void* d_out, int out_size, void* d_ws,
                              size_t ws_size, hipStream_t stream) {
  const unsigned short* x = (const unsigned short*)d_in[0];
  const char* kc = (const char*)d_in[1];
  const char* vc = (const char*)d_in[2];
  const char* wq = (const char*)d_in[3];
  const char* wo = (const char*)d_in[4];
  const char* w1 = (const char*)d_in[6];
  const unsigned short* b1 = (const unsigned short*)d_in[7];
  const char* w2 = (const char*)d_in[8];
  const unsigned short* b2 = (const unsigned short*)d_in[9];
  float* spart = (float*)d_ws;
  fused_attn_kernel<<<NB * NH, NT, 0, stream>>>(x, kc, vc, wq, wo, w1, b1, w2, spart);
  final_kernel<<<NB * 10, 256, 0, stream>>>(x, b2, spart, (unsigned short*)d_out);
}
```

```cpp
#define V_ADAPT 0
#include <hip/hip_runtime.h>
#include <hip/hip_bf16.h>
#include <stdint.h>

#define STAMP_DECL
#define STAMP(k) do { } while (0)
#define STAMP_FLUSH(base) do { } while (0)

#define V_WNT 1
#define V_SLP 16

namespace {

constexpr int NB = 8, DM = 640, NH = 32, SK = 4096, DH = 128, FF = 1728;
constexpr int NW = 8;
constexpr int NT = NW * 64;
constexpr int NHLP = 4, NSTR = 4;
constexpr int FS = FF / NH;
constexpr int QROWS = DM / NHLP;

typedef unsigned u32x4 __attribute__((ext_vector_type(4)));
typedef float f32x4 __attribute__((ext_vector_type(4)));
typedef float f32x2 __attribute__((ext_vector_type(2)));
typedef __bf16 bf16x2 __attribute__((ext_vector_type(2)));
typedef __bf16 bf16x8 __attribute__((ext_vector_type(8)));

__device__ __forceinline__ float bf_lo(unsigned u) { return __builtin_bit_cast(float, u << 16); }
__device__ __forceinline__ float bf_hi(unsigned u) { return __builtin_bit_cast(float, u & 0xffff0000u); }
__device__ __forceinline__ float bf1(unsigned short u) { return __builtin_bit_cast(float, (unsigned)u << 16); }
__device__ __forceinline__ float rbf(float x) { return (float)(__bf16)x; }
__device__ __forceinline__ unsigned short tobf(float x) { return __builtin_bit_cast(unsigned short, (__bf16)x); }
__device__ __forceinline__ unsigned pack2(float lo, float hi) { return (unsigned)tobf(lo) | ((unsigned)tobf(hi) << 16); }

__device__ __forceinline__ float dot2(unsigned k, unsigned q, float acc) {
  return __builtin_amdgcn_fdot2_f32_bf16(__builtin_bit_cast(bf16x2, k), __builtin_bit_cast(bf16x2, q), acc, false);
}
template <int CTRL> __device__ __forceinline__ float dpp(float x) {
  return __builtin_bit_cast(float, __builtin_amdgcn_mov_dpp(__builtin_bit_cast(int, x), CTRL, 0xf, 0xf, true));
}
__device__ __forceinline__ float row16_sum(float x) {
  x += dpp<0xB1>(x);
  x += dpp<0x4E>(x);
  x += dpp<0x141>(x);
  x += dpp<0x140>(x);
  return x;
}
__device__ __forceinline__ float xrow_sum(float x) {
  auto s = __builtin_amdgcn_permlane16_swap(__float_as_uint(x), __float_as_uint(x), false, false);
  x = __uint_as_float(s[0]) + __uint_as_float(s[1]);
  auto t = __builtin_amdgcn_permlane32_swap(__float_as_uint(x), __float_as_uint(x), false, false);
  return __uint_as_float(t[0]) + __uint_as_float(t[1]);
}
__device__ __forceinline__ float xrow_max(float x) {
  auto s = __builtin_amdgcn_permlane16_swap(__float_as_uint(x), __float_as_uint(x), false, false);
  x = fmaxf(__uint_as_float(s[0]), __uint_as_float(s[1]));
  auto t = __builtin_amdgcn_permlane32_swap(__float_as_uint(x), __float_as_uint(x), false, false);
  return fmaxf(__uint_as_float(t[0]), __uint_as_float(t[1]));
}
__device__ __forceinline__ float wave_sum(float x) { return xrow_sum(row16_sum(x)); }

__device__ __forceinline__ u32x4 ld_nt(const char* p) { return __builtin_nontemporal_load(reinterpret_cast<const u32x4*>(p)); }
__device__ __forceinline__ u32x4 ld_def(const char* p) { return *reinterpret_cast<const u32x4*>(p); }
__device__ __forceinline__ unsigned probe(const char* p) {
  unsigned long long t0, t1; unsigned v;
  asm volatile("s_memrealtime %0\n\ts_waitcnt lgkmcnt(0)\n\ts_load_dword %1, %3, 0x0\n\ts_waitcnt lgkmcnt(0)\n\ts_memrealtime %2\n\ts_waitcnt lgkmcnt(0)"
               : "=&s"(t0), "=&s"(v), "=&s"(t1) : "s"(p) : "memory");
  return (unsigned)(t1 - t0);
}
__device__ __forceinline__ u32x4 ld_w(const char* p) { return __builtin_nontemporal_load(reinterpret_cast<const u32x4*>(p)); }

__device__ __forceinline__ f32x4 gemv_mfma(u32x4 a, unsigned xdup, u32x4 msk, f32x4 c) {
  const u32x4 bsel = {xdup & msk[0], xdup & msk[1], xdup & msk[2], xdup & msk[3]};
  return __builtin_amdgcn_mfma_f32_16x16x32_bf16(__builtin_bit_cast(bf16x8, a), __builtin_bit_cast(bf16x8, bsel), c, 0, 0, 0);
}

constexpr int L_XQ = 0;
constexpr int L_X1 = L_XQ + DM;
constexpr int L_QP = L_X1 + DM;
constexpr int L_HP = L_QP + NHLP * DH;
constexpr int L_HA = L_HP + NHLP * 128;
constexpr int L_MG = L_HA + 64;
constexpr int MG_REC = DH + 4;
constexpr int L_PP = L_MG + NSTR * MG_REC;
constexpr int L_SC = L_PP + NHLP * 10 * 128;
constexpr int L_AL = L_SC + NSTR * 1024;
constexpr int L_CNT = L_AL + NSTR * 8;
constexpr int L_END = L_CNT + 4;

__global__ void __launch_bounds__(NT, 2) fused_attn_kernel(
    const unsigned short* __restrict__ x, const char* __restrict__ kc, const char* __restrict__ vc,
    const char* __restrict__ wq, const char* __restrict__ wo, const char* __restrict__ w1,
    const unsigned short* __restrict__ b1, const char* __restrict__ w2, float* __restrict__ spart) {
  __shared__ __attribute__((aligned(16))) unsigned lds[L_END];
  float* ldsf = reinterpret_cast<float*>(lds);
  const int tid = threadIdx.x;
  const unsigned lane = tid & 63;
  const int w = __builtin_amdgcn_readfirstlane(tid >> 6);
  const unsigned r = lane >> 4, c = lane & 15;
  const int bid = blockIdx.x;
  const int h = bid & 31, b = bid >> 5;
  STAMP_DECL
  STAMP(0);
#define SB __builtin_amdgcn_sched_barrier(0);
#define WAVE_LDS_SYNC __builtin_amdgcn_fence(__ATOMIC_RELEASE, "wavefront"); __builtin_amdgcn_wave_barrier(); __builtin_amdgcn_fence(__ATOMIC_ACQUIRE, "wavefront");
#define PACE SB __builtin_amdgcn_s_sleep(V_SLP); SB

  if (w >= NHLP) {
    const int j = w - NHLP;
    const char* kb = kc + (size_t)bid * (SK * DH * 2) + j * 4096;
    const char* vb = vc + (size_t)bid * (SK * DH * 2) + j * 4096;
    const unsigned lo16 = lane * 16;
    u32x4 R[8][4];
#pragma unroll
    for (int d = 0; d < 8; ++d) {
      const char* a_ = kb + d * 16384;
      asm volatile(
          "global_load_dwordx4 %0, %4, %5 nt\n\t"
          "global_load_dwordx4 %1, %4, %5 offset:1024 nt\n\t"
          "global_load_dwordx4 %2, %4, %5 offset:2048 nt\n\t"
          "global_load_dwordx4 %3, %4, %5 offset:3072 nt"
          : "=&v"(R[d][0]), "=&v"(R[d][1]), "=&v"(R[d][2]), "=&v"(R[d][3]) : "v"(lo16), "s"(a_) : "memory");
    }
    STAMP(1);
    __syncthreads();
    STAMP(2);
    u32x4 q;
    {
      const float* qp = &ldsf[L_QP + r * DH + c * 8];
      f32x4 s0 = *reinterpret_cast<const f32x4*>(qp);
      f32x4 s1 = *reinterpret_cast<const f32x4*>(qp + 4);
#pragma unroll
      for (int k = 0; k < 4; ++k) { s0[k] = xrow_sum(s0[k]); s1[k] = xrow_sum(s1[k]); }
      q[0] = pack2(s0[0], s0[1]); q[1] = pack2(s0[2], s0[3]); q[2] = pack2(s1[0], s1[1]); q[3] = pack2(s1[2], s1[3]);
    }
    asm volatile("" : "+v"(q));
    STAMP(3);

    constexpr float C2 = 0.08838834764831845f * 1.4426950408889634f;
    float* scw = &ldsf[L_SC + j * 1024];
    float* alw = &ldsf[L_AL + j * 8];
    const bool c1 = (c & 1) != 0, c2 = (c & 2) != 0;
    float m = -INFINITY, l = 0.f;
    float acc[8];
#pragma unroll
    for (int k = 0; k < 8; ++k) acc[k] = 0.f;

#define RING_WAIT(d, N) asm volatile("s_waitcnt vmcnt(" #N ")" : "+v"(R[d][0]), "+v"(R[d][1]), "+v"(R[d][2]), "+v"(R[d][3]) : : "memory");
#define REFILL(d)                                                                                     \
  {                                                                                                   \
    const char* a_ = nb + (d) * 16384;                                                                \
    asm volatile(                                                                                     \
        "s_cmp_lg_u32 %6, 0\n\t"                                                                      \
        "s_cbranch_scc1 1f\n\t"                                                                       \
        "global_load_dwordx4 %0, %4, %5 nt\n\t"                                                       \
        "global_load_dwordx4 %1, %4, %5 offset:1024 nt\n\t"                                           \
        "global_load_dwordx4 %2, %4, %5 offset:2048 nt\n\t"                                           \
        "global_load_dwordx4 %3, %4, %5 offset:3072 nt\n\t"                                           \
        "s_branch 2f\n"                                                                               \
        "1:\n\t"                                                                                      \
        "global_load_dwordx4 %0, %4, %5\n\t"                                                          \
        "global_load_dwordx4 %1, %4, %5 offset:1024\n\t"                                              \
        "global_load_dwordx4 %2, %4, %5 offset:2048\n\t"                                              \
        "global_load_dwordx4 %3, %4, %5 offset:3072\n"                                                \
        "2:"                                                                                          \
        : "+v"(R[d][0]), "+v"(R[d][1]), "+v"(R[d][2]), "+v"(R[d][3])                                  \
        : "v"(lo16), "s"(a_), "s"(dflt) : "scc", "memory");                                           \
  }
#define QK_PIECE(d)                                                                                   \
  {                                                                                                   \
    float s_[4];                                                                                      \
    _Pragma("unroll") for (int t = 0; t < 4; ++t) {                                                   \
      float dd = 0.f;                                                                                 \
      _Pragma("unroll") for (int k = 0; k < 4; ++k) dd = dot2(R[d][t][k], q[k], dd);                  \
      s_[t] = dd;                                                                                     \
    }                                                                                                 \
            \
    const float x0 = (c1 ? s_[1] : s_[0]) + dpp<0xB1>(c1 ? s_[0] : s_[1]);                            \
    const float x1 = (c1 ? s_[3] : s_[2]) + dpp<0xB1>(c1 ? s_[2] : s_[3]);                            \
    float y = (c2 ? x1 : x0) + dpp<0x4E>(c2 ? x0 : x1);                                               \
    y += dpp<0x128>(y);                                                                               \
    y += dpp<0x124>(y);                                                                               \
    mx = fmaxf(mx, y);                                                                                \
    if (c < 4) sb[r * 4 + (c & 3) + (d) * 16] = y;                                                    \
  }
#define PV_PIECE(d)                                                                                   \
  {                                                                                                   \
    const f32x4 p_ = *reinterpret_cast<const f32x4*>(sb + r * 4 + (d) * 16);                          \
    _Pragma("unroll") for (int t = 0; t < 4; ++t) {                                                   \
      _Pragma("unroll") for (int k = 0; k < 4; ++k) {                                                 \
        acc[2 * k] = fmaf(p_[t], bf_lo(R[d][t][k]), acc[2 * k]);                                      \
        acc[2 * k + 1] = fmaf(p_[t], bf_hi(R[d][t][k]), acc[2 * k + 1]);                              \
      }                                                                                               \
    }                                                                                                 \
  }
#define PIECE(d) RING_WAIT(d, 28) if (curv) PV_PIECE(d) else QK_PIECE(d) REFILL(d)
    {
      int kproc = 0, kn = 1, vn = 0;
      bool curv = false;
      int ci = 0;
      for (;;) {
        const bool res = V_ADAPT && __builtin_amdgcn_readfirstlane(
                             __hip_atomic_load(&lds[L_CNT + 2], __ATOMIC_RELAXED, __HIP_MEMORY_SCOPE_WORKGROUP)) == 3u;
        if (!curv) ++kproc;
        const bool nv = (vn < kproc) && ((V_ADAPT > 1 && res) || kn == 8);
        if (!nv && kn == 8) break;
        const int ni = nv ? vn++ : kn++;
        const char* nb = (nv ? vb : kb) + ni * 131072;
        const unsigned dflt = __builtin_amdgcn_readfirstlane((nv && res) ? 1u : 0u);
        float* sb = scw + ci * 128;
        float mx = -INFINITY;
        if (curv) {
          const float alpha = alw[ci];
#pragma unroll
          for (int k = 0; k < 8; ++k) acc[k] *= alpha;
        }
        PIECE(0) PIECE(1) PIECE(2) PIECE(3) PIECE(4) PIECE(5) PIECE(6) PIECE(7)
        if (!curv) {
          mx = fmaxf(mx, dpp<0xB1>(mx));
          mx = fmaxf(mx, dpp<0x4E>(mx));
          const float mn = fmaxf(m, xrow_max(mx) * C2);
          const float alpha = __builtin_amdgcn_exp2f(m - mn);
          m = mn;
          WAVE_LDS_SYNC
          f32x2 v2 = *reinterpret_cast<const f32x2*>(sb + lane * 2);
          v2[0] = __builtin_amdgcn_exp2f(fmaf(v2[0], C2, -mn));
          v2[1] = __builtin_amdgcn_exp2f(fmaf(v2[1], C2, -mn));
          l = fmaf(l, alpha, v2[0] + v2[1]);
          *reinterpret_cast<f32x2*>(sb + lane * 2) = v2;
          if (lane == 0) alw[ci] = alpha;
          WAVE_LDS_SYNC
        }
        curv = nv;
        ci = ni;
      }
      const float* sb = scw + 7 * 128;
      const float alpha = alw[7];
#pragma unroll
      for (int k = 0; k < 8; ++k) acc[k] *= alpha;
      RING_WAIT(0, 28) PV_PIECE(0) RING_WAIT(1, 24) PV_PIECE(1) RING_WAIT(2, 20) PV_PIECE(2) RING_WAIT(3, 16) PV_PIECE(3)
      RING_WAIT(4, 12) PV_PIECE(4) RING_WAIT(5, 8) PV_PIECE(5) RING_WAIT(6, 4) PV_PIECE(6) RING_WAIT(7, 0) PV_PIECE(7)
    }
    l = wave_sum(l);
    STAMP(4);
    {
#pragma unroll
      for (int k = 0; k < 8; ++k) acc[k] = xrow_sum(acc[k]);
      float* rec = &ldsf[L_MG + j * MG_REC];
      if (r == 0) {
        *reinterpret_cast<f32x4*>(rec + 4 + c * 8) = f32x4{acc[0], acc[1], acc[2], acc[3]};
        *reinterpret_cast<f32x4*>(rec + 4 + c * 8 + 4) = f32x4{acc[4], acc[5], acc[6], acc[7]};
        if (c == 0) { rec[0] = m; rec[1] = l; }
      }
    }
    __syncthreads();
    STAMP(5);
    __syncthreads();
    STAMP(6);
  } else {
    __builtin_amdgcn_s_setprio(3);
    const int js = (h & 7) * 4 + (h >> 3);
    const unsigned r8 = lane >> 3, c8 = lane & 7;
    const int chunk0 = (FS * js) >> 3;
    const unsigned short* xb = x + b * DM;
    const u32x4 xa = *reinterpret_cast<const u32x4*>(xb + lane * 8);
    const unsigned xt = *reinterpret_cast<const unsigned*>(xb + 512 + lane * 2);
    const unsigned short xo0 = xb[QROWS * w + lane];
    const unsigned short xo1 = xb[QROWS * w + 64 + lane];
    const unsigned short xo2 = xb[QROWS * w + 128 + (lane & 31)];
    const unsigned short b1u = b1[FS * js + min(lane, (unsigned)(FS - 1))];
    SB
    u32x4 wr[40];
    {
      const char* p = wq + (size_t)(QROWS * w) * (NH * DH * 2) + h * (DH * 2);
      const unsigned lo = r * (NH * DH * 2) + c * 16;
#pragma unroll
      for (int i = 0; i < 40; ++i) {
        wr[i] = ld_w(p + (size_t)(4 * i) * (NH * DH * 2) + lo);
        if ((i & 3) == 0) { SB }
      }
    }
    SB
    if (tid == 0) { lds[L_CNT + 1] = 0; lds[L_CNT + 2] = 0; }
    u32x4 msk8, msk16;
#pragma unroll
    for (int k = 0; k < 4; ++k) {
      const unsigned m16 = (c8 == 2u * k) ? 0x0000ffffu : ((c8 == 2u * k + 1) ? 0xffff0000u : 0u);
      msk16[k] = m16;
      msk8[k] = (c < 8) ? m16 : 0u;
    }
    {
      float xv[10];
#pragma unroll
      for (int k = 0; k < 4; ++k) { xv[2 * k] = bf_lo(xa[k]); xv[2 * k + 1] = bf_hi(xa[k]); }
      xv[8] = bf_lo(xt); xv[9] = bf_hi(xt);
      float sm = 0.f;
#pragma unroll
      for (int k = 0; k < 10; ++k) sm += xv[k];
      const float mean = wave_sum(sm) * (1.0f / DM);
      float ss = 0.f;
#pragma unroll
      for (int k = 0; k < 10; ++k) { const float d = xv[k] - mean; ss = fmaf(d, d, ss); }
      const float var = wave_sum(ss) * (1.0f / DM);
      const float mb = rbf(mean);
      const float sd = rbf(sqrtf(rbf(rbf(var) + 1e-5f)));
      const unsigned n0 = tobf(rbf(bf1(xo0) - mb) / sd);
      const unsigned n1 = tobf(rbf(bf1(xo1) - mb) / sd);
      const unsigned n2 = tobf(rbf(bf1(xo2) - mb) / sd);
      const unsigned d0 = n0 | (n0 << 16), d1 = n1 | (n1 << 16), d2 = n2 | (n2 << 16);
      unsigned* xq = &lds[L_XQ + w * QROWS + (lane & 3) * 40 + (lane >> 2)];
      unsigned* x1 = &lds[L_X1 + w * QROWS + (lane & 7) * 20 + (lane >> 3)];
      xq[0] = d0; xq[16] = d1;
      x1[0] = d0; x1[8] = d1;
      if (lane < 32) { xq[32] = d2; x1[16] = d2; }
    }
    __builtin_amdgcn_fence(__ATOMIC_RELEASE, "wavefront");
    __builtin_amdgcn_wave_barrier();
    __builtin_amdgcn_fence(__ATOMIC_ACQUIRE, "wavefront");
    {
      f32x4 a0 = {0.f, 0.f, 0.f, 0.f}, a1 = {0.f, 0.f, 0.f, 0.f};
#pragma unroll
      for (int i4 = 0; i4 < 10; ++i4) {
        const u32x4 xd = *reinterpret_cast<const u32x4*>(&lds[L_XQ + w * QROWS + r * 40 + i4 * 4]);
        a0 = gemv_mfma(wr[4 * i4 + 0], xd[0], msk8, a0);
        a1 = gemv_mfma(wr[4 * i4 + 1], xd[1], msk8, a1);
        a0 = gemv_mfma(wr[4 * i4 + 2], xd[2], msk8, a0);
        a1 = gemv_mfma(wr[4 * i4 + 3], xd[3], msk8, a1);
      }
      a0 += a1;
      if (c < 8) {
#pragma unroll
        for (int g = 0; g < 4; ++g) ldsf[L_QP + w * DH + 8 * (4 * r + g) + c] = a0[g];
      }
    }
    __builtin_amdgcn_s_setprio(0);
    STAMP(1);
    __syncthreads();
    STAMP(2);
    {
      const unsigned ch = min((unsigned)chunk0 + c8, (unsigned)(FF / 8 - 1));
      const char* p = w1 + (size_t)(QROWS * w) * (FF * 2);
      const unsigned lo = r8 * (FF * 2) + ch * 16;
#pragma unroll
      for (int i = 0; i < 20; ++i) { wr[i] = ld_w(p + (size_t)(8 * i) * (FF * 2) + lo); PACE }
      SB
      const char* p2 = w2 + (size_t)(FS * js) * (DM * 2);
#pragma unroll
      for (int g = 0; g < 4; ++g) {
        const unsigned lo2 = min(16u * w + 4 * g + r, (unsigned)(FS - 1)) * (DM * 2) + c * 16;
#pragma unroll
        for (int s = 0; s < 5; ++s) { wr[20 + 5 * g + s] = ld_w(p2 + s * 256 + lo2); PACE }
      }
      SB
      f32x4 a0 = {0.f, 0.f, 0.f, 0.f}, a1 = {0.f, 0.f, 0.f, 0.f};
#pragma unroll
      for (int i4 = 0; i4 < 5; ++i4) {
        const u32x4 xd = *reinterpret_cast<const u32x4*>(&lds[L_X1 + w * QROWS + r8 * 20 + i4 * 4]);
        a0 = gemv_mfma(wr[4 * i4 + 0], xd[0], msk16, a0);
        a1 = gemv_mfma(wr[4 * i4 + 1], xd[1], msk16, a1);
        a0 = gemv_mfma(wr[4 * i4 + 2], xd[2], msk16, a0);
        a1 = gemv_mfma(wr[4 * i4 + 3], xd[3], msk16, a1);
      }
      a0 += a1;
      if ((r >> 1) == (c >> 3)) {
#pragma unroll
        for (int g = 0; g < 4; ++g) ldsf[L_HP + w * 128 + (r >> 1) * 64 + 8 * (4 * (r & 1) + g) + c8] = a0[g];
      }
      __builtin_amdgcn_fence(__ATOMIC_RELEASE, "workgroup", "local");
      if (lane == 0) atomicAdd(&lds[L_CNT + 1], 1u);
      while (__hip_atomic_load(&lds[L_CNT + 1], __ATOMIC_RELAXED, __HIP_MEMORY_SCOPE_WORKGROUP) < (unsigned)NHLP) __builtin_amdgcn_s_sleep(2);
      __builtin_amdgcn_fence(__ATOMIC_ACQUIRE, "workgroup", "local");
      unsigned hd = 0u;
      {
        const unsigned off = FS * js - chunk0 * 8;
        float sm = 0.f;
#pragma unroll
        for (int ww = 0; ww < 2 * NHLP; ++ww) sm += ldsf[L_HP + ww * 64 + ((off + lane) & 63)];
        const float hb = rbf(rbf(sm) + bf1(b1u));
        const float u2 = 1.5957691216057308f * (hb + 0.044715f * hb * hb * hb);
        const unsigned hv = tobf(hb / (1.0f + __expf(-u2)));
        hd = (lane < (unsigned)FS) ? (hv | (hv << 16)) : 0u;
      }
      unsigned hg[4];
#pragma unroll
      for (int g = 0; g < 4; ++g) hg[g] = __builtin_amdgcn_ds_bpermute((int)((16 * w + 4 * g + r) * 4), (int)hd);
#pragma unroll
      for (int s = 0; s < 5; ++s) {
        f32x4 mm = {0.f, 0.f, 0.f, 0.f};
#pragma unroll
        for (int g = 0; g < 4; ++g) mm = gemv_mfma(wr[20 + 5 * g + s], hg[g], msk8, mm);
        if (c < 8) *reinterpret_cast<f32x4*>(&ldsf[L_PP + ((w * 10 + 5 + s) * 32 + r * 8 + c) * 4]) = mm;
      }
    }
    SB
    {
      const char* wop = wo + (size_t)(h * DH + 32 * w) * (DM * 2);
      const unsigned wlo = r * (DM * 2) + c * 16;
#pragma unroll
      for (int u = 0; u < 40; ++u) { wr[u] = ld_w(wop + (u / 5) * (4 * DM * 2) + (u % 5) * 256 + wlo); PACE }
    }
    SB
    STAMP(3);
    __syncthreads();
    STAMP(5);
    unsigned od[8];
    {
      float mv[NSTR], lv[NSTR], av[NSTR];
#pragma unroll
      for (int ww = 0; ww < NSTR; ++ww) {
        const float* rec = &ldsf[L_MG + ww * MG_REC];
        mv[ww] = rec[0]; lv[ww] = rec[1]; av[ww] = rec[4 + 32 * w + (lane & 31)];
      }
      float M = mv[0];
#pragma unroll
      for (int ww = 1; ww < NSTR; ++ww) M = fmaxf(M, mv[ww]);
      float L = 0.f, o = 0.f;
#pragma unroll
      for (int ww = 0; ww < NSTR; ++ww) {
        const float wt = __builtin_amdgcn_exp2f(mv[ww] - M);
        L = fmaf(wt, lv[ww], L);
        o = fmaf(wt, av[ww], o);
      }
      const unsigned ob = tobf(o / L);
      const unsigned odup = ob | (ob << 16);
#pragma unroll
      for (int g = 0; g < 8; ++g) od[g] = __builtin_amdgcn_ds_bpermute((int)((4 * g + r) * 4), (int)odup);
    }
    {
      f32x4 a[5];
#pragma unroll
      for (int s = 0; s < 5; ++s) a[s] = f32x4{0.f, 0.f, 0.f, 0.f};
#pragma unroll
      for (int g = 0; g < 8; ++g) {
#pragma unroll
        for (int s = 0; s < 5; ++s) a[s] = gemv_mfma(wr[5 * g + s], od[g], msk8, a[s]);
      }
      if (c < 8) {
#pragma unroll
        for (int s = 0; s < 5; ++s) *reinterpret_cast<f32x4*>(&ldsf[L_PP + ((w * 10 + s) * 32 + r * 8 + c) * 4]) = a[s];
      }
    }
    __syncthreads();
    STAMP(6);
  }
  if (tid < 320) {
    f32x4 s = {0.f, 0.f, 0.f, 0.f};
#pragma unroll
    for (int ww = 0; ww < NHLP; ++ww) s += *reinterpret_cast<const f32x4*>(&ldsf[L_PP + (ww * 320 + tid) * 4]);
    *reinterpret_cast<f32x4*>(spart + (size_t)bid * 1280 + tid * 4) = s;
  }
  STAMP(7);
  STAMP_FLUSH(0);
}

__global__ void __launch_bounds__(256) final_kernel(const unsigned short* __restrict__ x, const unsigned short* __restrict__ b2,
                                                    const float* __restrict__ spart, unsigned short* __restrict__ out) {
  __shared__ __attribute__((aligned(16))) float red[2][16][64];
  const int b = blockIdx.x / 10, seg = blockIdx.x % 10;
  STAMP_DECL
  STAMP(0);
  const int tid = threadIdx.x, cq = tid & 15, g = tid >> 4;
  const size_t base = (size_t)(b * NH + 2 * g) * 1280 + seg * 64 + cq * 4;
  const f32x4 s0 = *reinterpret_cast<const f32x4*>(spart + base);
  const f32x4 s1 = *reinterpret_cast<const f32x4*>(spart + base + 1280);
  const f32x4 m0 = *reinterpret_cast<const f32x4*>(spart + base + 640);
  const f32x4 m1 = *reinterpret_cast<const f32x4*>(spart + base + 1280 + 640);
  const int p = seg * 64 + (tid & 63);
  const int n = (p >> 7) * 128 + 8 * (4 * ((p >> 5) & 3) + (p & 3)) + ((p >> 2) & 7);
  float b2v = bf1(b2[n]);
  float xv = bf1(x[b * DM + n]);
  asm volatile("" : "+v"(b2v), "+v"(xv));
  *reinterpret_cast<f32x4*>(&red[0][g][cq * 4]) = s0 + s1;
  *reinterpret_cast<f32x4*>(&red[1][g][cq * 4]) = m0 + m1;
  __syncthreads();
  if (tid < 64) {
    float s = 0.f, mm = 0.f;
#pragma unroll
    for (int k = 0; k < 16; ++k) { s += red[0][k][tid]; mm += red[1][k][tid]; }
    const float sb = rbf(s);
    const float mb = rbf(rbf(mm) + b2v);
    const float gt = rbf(mb / (1.0f + __expf(-mb)));
    const float res = xv + rbf(sb * gt);
    out[b * DM + n] = tobf(res);
  }
  STAMP(1);
  STAMP_FLUSH(256);
}

}

extern "C" void kernel_launch(void* const* d_in, const int* in_sizes, int n_in, void* d_out, int out_size, void* d_ws,
                              size_t ws_size, hipStream_t stream) {
  const unsigned short* x = (const unsigned short*)d_in[0];
  const char* kc = (const char*)d_in[1];
  const char* vc = (const char*)d_in[2];
  const char* wq = (const char*)d_in[3];
  const char* wo = (const char*)d_in[4];
  const char* w1 = (const char*)d_in[6];
  const unsigned short* b1 = (const unsigned short*)d_in[7];
  const char* w2 = (const char*)d_in[8];
  const unsigned short* b2 = (const unsigned short*)d_in[9];
  float* spart = (float*)d_ws;
  fused_attn_kernel<<<NB * NH, NT, 0, stream>>>(x, kc, vc, wq, wo, w1, b1, w2, spart);
  final_kernel<<<NB * 10, 256, 0, stream>>>(x, b2, spart, (unsigned short*)d_out);
}
```

```cpp
#include <hip/hip_runtime.h>
#include <hip/hip_bf16.h>
#include <stdint.h>

namespace {

constexpr int NB = 8, DM = 640, NH = 32, SK = 4096, DH = 128, FF = 1728;
constexpr int NW = 8;
constexpr int NT = NW * 64;
constexpr int NHLP = 4, NSTR = 4;
constexpr int FS = FF / NH;
constexpr int QROWS = DM / NHLP;

typedef unsigned u32x4 __attribute__((ext_vector_type(4)));
typedef float f32x4 __attribute__((ext_vector_type(4)));
typedef __bf16 bf16x2 __attribute__((ext_vector_type(2)));
typedef __bf16 bf16x8 __attribute__((ext_vector_type(8)));

__device__ __forceinline__ float bf_lo(unsigned u) { return __builtin_bit_cast(float, u << 16); }
__device__ __forceinline__ float bf_hi(unsigned u) { return __builtin_bit_cast(float, u & 0xffff0000u); }
__device__ __forceinline__ float bf1(unsigned short u) { return __builtin_bit_cast(float, (unsigned)u << 16); }
__device__ __forceinline__ float rbf(float x) { return (float)(__bf16)x; }
__device__ __forceinline__ unsigned short tobf(float x) { return __builtin_bit_cast(unsigned short, (__bf16)x); }
__device__ __forceinline__ unsigned pack2(float lo, float hi) { return (unsigned)tobf(lo) | ((unsigned)tobf(hi) << 16); }

__device__ __forceinline__ float dot2(unsigned k, unsigned q, float acc) {
  return __builtin_amdgcn_fdot2_f32_bf16(__builtin_bit_cast(bf16x2, k), __builtin_bit_cast(bf16x2, q), acc, false);
}
template <int CTRL> __device__ __forceinline__ float dpp(float x) {
  return __builtin_bit_cast(float, __builtin_amdgcn_mov_dpp(__builtin_bit_cast(int, x), CTRL, 0xf, 0xf, true));
}
__device__ __forceinline__ float row16_sum(float x) {
  x += dpp<0xB1>(x);
  x += dpp<0x4E>(x);
  x += dpp<0x141>(x);
  x += dpp<0x140>(x);
  return x;
}
__device__ __forceinline__ float xrow_sum(float x) {
  auto s = __builtin_amdgcn_permlane16_swap(__float_as_uint(x), __float_as_uint(x), false, false);
  x = __uint_as_float(s[0]) + __uint_as_float(s[1]);
  auto t = __builtin_amdgcn_permlane32_swap(__float_as_uint(x), __float_as_uint(x), false, false);
  return __uint_as_float(t[0]) + __uint_as_float(t[1]);
}
__device__ __forceinline__ float xrow_max(float x) {
  auto s = __builtin_amdgcn_permlane16_swap(__float_as_uint(x), __float_as_uint(x), false, false);
  x = fmaxf(__uint_as_float(s[0]), __uint_as_float(s[1]));
  auto t = __builtin_amdgcn_permlane32_swap(__float_as_uint(x), __float_as_uint(x), false, false);
  return fmaxf(__uint_as_float(t[0]), __uint_as_float(t[1]));
}
__device__ __forceinline__ float wave_sum(float x) { return xrow_sum(row16_sum(x)); }

__device__ __forceinline__ u32x4 ld_nt(const char* p) { return __builtin_nontemporal_load(reinterpret_cast<const u32x4*>(p)); }
__device__ __forceinline__ u32x4 ld_w(const char* p) { return __builtin_nontemporal_load(reinterpret_cast<const u32x4*>(p)); }

__device__ __forceinline__ f32x4 gemv_mfma(u32x4 a, unsigned xdup, u32x4 msk, f32x4 c) {
  const u32x4 bsel = {xdup & msk[0], xdup & msk[1], xdup & msk[2], xdup & msk[3]};
  return __builtin_amdgcn_mfma_f32_16x16x32_bf16(__builtin_bit_cast(bf16x8, a), __builtin_bit_cast(bf16x8, bsel), c, 0, 0, 0);
}

constexpr int L_XQ = 0;
constexpr int L_X1 = L_XQ + DM;
constexpr int L_QP = L_X1 + DM;
constexpr int L_HP = L_QP + NHLP * DH;
constexpr int L_MG = L_HP + NHLP * 128;
constexpr int MG_REC = DH + 4;
constexpr int L_PP = L_MG + NSTR * MG_REC;
constexpr int L_SC = L_PP + NHLP * 10 * 128;
constexpr int L_CNT = L_SC + NSTR * 1024;
constexpr int L_END = L_CNT + 4;

__global__ void __launch_bounds__(NT, 2) fused_attn_kernel(
    const unsigned short* __restrict__ x, const char* __restrict__ kc, const char* __restrict__ vc,
    const char* __restrict__ wq, const char* __restrict__ wo, const char* __restrict__ w1,
    const unsigned short* __restrict__ b1, const char* __restrict__ w2, float* __restrict__ spart) {
  __shared__ __attribute__((aligned(16))) unsigned lds[L_END];
  float* ldsf = reinterpret_cast<float*>(lds);
  const int tid = threadIdx.x;
  const unsigned lane = tid & 63;
  const int w = __builtin_amdgcn_readfirstlane(tid >> 6);
  const unsigned r = lane >> 4, c = lane & 15;
  const int bid = blockIdx.x;
  const int h = bid & 31, b = bid >> 5;
#define SB __builtin_amdgcn_sched_barrier(0);
#define PACE SB __builtin_amdgcn_s_sleep(16); SB

  if (w >= NHLP) {
    const int j = w - NHLP;
    const char* kb = kc + (size_t)bid * (SK * DH * 2) + j * 4096;
    const char* vb = vc + (size_t)bid * (SK * DH * 2) + j * 4096;
    const unsigned lo16 = lane * 16;
    u32x4 R[8][4];
#define LOAD_P(d, base) _Pragma("unroll") for (int t = 0; t < 4; ++t) R[d][t] = ld_nt((base) + (d) * 16384 + t * 1024 + lo16);
#pragma unroll
    for (int d = 0; d < 8; ++d) { LOAD_P(d, kb) SB }
    __syncthreads();
    u32x4 q;
    {
      const float* qp = &ldsf[L_QP + r * DH + c * 8];
      f32x4 s0 = *reinterpret_cast<const f32x4*>(qp);
      f32x4 s1 = *reinterpret_cast<const f32x4*>(qp + 4);
#pragma unroll
      for (int k = 0; k < 4; ++k) { s0[k] = xrow_sum(s0[k]); s1[k] = xrow_sum(s1[k]); }
      q[0] = pack2(s0[0], s0[1]); q[1] = pack2(s0[2], s0[3]); q[2] = pack2(s1[0], s1[1]); q[3] = pack2(s1[2], s1[3]);
    }
    asm volatile("" : "+v"(q));

    float* scw = &ldsf[L_SC + j * 1024];
    const bool c1 = (c & 1) != 0, c2 = (c & 2) != 0;
    float mx = -INFINITY;
    {
      float* sp = scw + r * 4 + (c & 3);
      const char* nb = kb + 8 * 16384;
      for (int it = 0; it < 8; ++it) {
        if (it == 7) nb = vb;
#pragma unroll
        for (int d = 0; d < 8; ++d) {
          float s_[4];
#pragma unroll
          for (int t = 0; t < 4; ++t) {
            float dd = 0.f;
#pragma unroll
            for (int k = 0; k < 4; ++k) dd = dot2(R[d][t][k], q[k], dd);
            s_[t] = dd;
          }
          asm volatile("" : "+v"(s_[0]), "+v"(s_[1]), "+v"(s_[2]), "+v"(s_[3]));
          LOAD_P(d, nb)
          SB
          const float x0 = (c1 ? s_[1] : s_[0]) + dpp<0xB1>(c1 ? s_[0] : s_[1]);
          const float x1 = (c1 ? s_[3] : s_[2]) + dpp<0xB1>(c1 ? s_[2] : s_[3]);
          float y = (c2 ? x1 : x0) + dpp<0x4E>(c2 ? x0 : x1);
          y += dpp<0x128>(y);
          y += dpp<0x124>(y);
          mx = fmaxf(mx, y);
          if (c < 4) sp[d * 16] = y;
          SB
        }
        sp += 128;
        nb += 8 * 16384;
      }
    }
    constexpr float C2 = 0.08838834764831845f * 1.4426950408889634f;
    float m, l;
    {
      mx = fmaxf(mx, dpp<0xB1>(mx));
      mx = fmaxf(mx, dpp<0x4E>(mx));
      m = xrow_max(mx) * C2;
      __builtin_amdgcn_fence(__ATOMIC_RELEASE, "wavefront");
      __builtin_amdgcn_wave_barrier();
      __builtin_amdgcn_fence(__ATOMIC_ACQUIRE, "wavefront");
      float ls = 0.f;
#pragma unroll
      for (int u = 0; u < 4; ++u) {
        f32x4 v = *reinterpret_cast<const f32x4*>(scw + u * 256 + lane * 4);
#pragma unroll
        for (int k = 0; k < 4; ++k) v[k] = __builtin_amdgcn_exp2f(fmaf(v[k], C2, -m));
        ls += (v[0] + v[1]) + (v[2] + v[3]);
        *reinterpret_cast<f32x4*>(scw + u * 256 + lane * 4) = v;
      }
      l = wave_sum(ls);
      __builtin_amdgcn_fence(__ATOMIC_RELEASE, "wavefront");
      __builtin_amdgcn_wave_barrier();
      __builtin_amdgcn_fence(__ATOMIC_ACQUIRE, "wavefront");
    }
    float acc[8];
#pragma unroll
    for (int k = 0; k < 8; ++k) acc[k] = 0.f;
#define PV_PIECE(d, pp)                                               \
  {                                                                   \
    const f32x4 p_ = *reinterpret_cast<const f32x4*>((pp) + (d) * 16); \
    _Pragma("unroll") for (int t = 0; t < 4; ++t) {                   \
      _Pragma("unroll") for (int k = 0; k < 4; ++k) {                 \
        acc[2 * k] = fmaf(p_[t], bf_lo(R[d][t][k]), acc[2 * k]);      \
        acc[2 * k + 1] = fmaf(p_[t], bf_hi(R[d][t][k]), acc[2 * k + 1]); \
      }                                                               \
    }                                                                 \
    asm volatile("" : "+v"(acc[0]), "+v"(acc[1]), "+v"(acc[2]), "+v"(acc[3]), "+v"(acc[4]), "+v"(acc[5]), "+v"(acc[6]), "+v"(acc[7])); \
  }
    {
      const float* pp = scw + r * 4;
      const char* nb = vb + 8 * 16384;
      for (int it = 0; it < 7; ++it) {
#pragma unroll
        for (int d = 0; d < 8; ++d) { PV_PIECE(d, pp) LOAD_P(d, nb) SB }
        pp += 128;
        nb += 8 * 16384;
      }
#pragma unroll
      for (int d = 0; d < 8; ++d) { PV_PIECE(d, pp) SB }
    }
    {
#pragma unroll
      for (int k = 0; k < 8; ++k) acc[k] = xrow_sum(acc[k]);
      float* rec = &ldsf[L_MG + j * MG_REC];
      if (r == 0) {
        *reinterpret_cast<f32x4*>(rec + 4 + c * 8) = f32x4{acc[0], acc[1], acc[2], acc[3]};
        *reinterpret_cast<f32x4*>(rec + 4 + c * 8 + 4) = f32x4{acc[4], acc[5], acc[6], acc[7]};
        if (c == 0) { rec[0] = m; rec[1] = l; }
      }
    }
    __syncthreads();
    __syncthreads();
  } else {
    __builtin_amdgcn_s_setprio(3);
    const int js = (h & 7) * 4 + (h >> 3);
    const unsigned r8 = lane >> 3, c8 = lane & 7;
    const int chunk0 = (FS * js) >> 3;
    const unsigned short* xb = x + b * DM;
    const u32x4 xa = *reinterpret_cast<const u32x4*>(xb + lane * 8);
    const unsigned xt = *reinterpret_cast<const unsigned*>(xb + 512 + lane * 2);
    const unsigned short xo0 = xb[QROWS * w + lane];
    const unsigned short xo1 = xb[QROWS * w + 64 + lane];
    const unsigned short xo2 = xb[QROWS * w + 128 + (lane & 31)];
    const unsigned short b1u = b1[FS * js + min(lane, (unsigned)(FS - 1))];
    SB
    u32x4 wr[40];
    {
      const char* p = wq + (size_t)(QROWS * w) * (NH * DH * 2) + h * (DH * 2);
      const unsigned lo = r * (NH * DH * 2) + c * 16;
#pragma unroll
      for (int i = 0; i < 40; ++i) {
        wr[i] = ld_w(p + (size_t)(4 * i) * (NH * DH * 2) + lo);
        if ((i & 3) == 0) { SB }
      }
    }
    SB
    if (tid == 0) lds[L_CNT + 1] = 0;
    u32x4 msk8, msk16;
#pragma unroll
    for (int k = 0; k < 4; ++k) {
      const unsigned m16 = (c8 == 2u * k) ? 0x0000ffffu : ((c8 == 2u * k + 1) ? 0xffff0000u : 0u);
      msk16[k] = m16;
      msk8[k] = (c < 8) ? m16 : 0u;
    }
    {
      float xv[10];
#pragma unroll
      for (int k = 0; k < 4; ++k) { xv[2 * k] = bf_lo(xa[k]); xv[2 * k + 1] = bf_hi(xa[k]); }
      xv[8] = bf_lo(xt); xv[9] = bf_hi(xt);
      float sm = 0.f;
#pragma unroll
      for (int k = 0; k < 10; ++k) sm += xv[k];
      const float mean = wave_sum(sm) * (1.0f / DM);
      float ss = 0.f;
#pragma unroll
      for (int k = 0; k < 10; ++k) { const float d = xv[k] - mean; ss = fmaf(d, d, ss); }
      const float var = wave_sum(ss) * (1.0f / DM);
      const float mb = rbf(mean);
      const float sd = rbf(sqrtf(rbf(rbf(var) + 1e-5f)));
      const unsigned n0 = tobf(rbf(bf1(xo0) - mb) / sd);
      const unsigned n1 = tobf(rbf(bf1(xo1) - mb) / sd);
      const unsigned n2 = tobf(rbf(bf1(xo2) - mb) / sd);
      const unsigned d0 = n0 | (n0 << 16), d1 = n1 | (n1 << 16), d2 = n2 | (n2 << 16);
      unsigned* xq = &lds[L_XQ + w * QROWS + (lane & 3) * 40 + (lane >> 2)];
      unsigned* x1 = &lds[L_X1 + w * QROWS + (lane & 7) * 20 + (lane >> 3)];
      xq[0] = d0; xq[16] = d1;
      x1[0] = d0; x1[8] = d1;
      if (lane < 32) { xq[32] = d2; x1[16] = d2; }
    }
    __builtin_amdgcn_fence(__ATOMIC_RELEASE, "wavefront");
    __builtin_amdgcn_wave_barrier();
    __builtin_amdgcn_fence(__ATOMIC_ACQUIRE, "wavefront");
    {
      f32x4 a0 = {0.f, 0.f, 0.f, 0.f}, a1 = {0.f, 0.f, 0.f, 0.f};
#pragma unroll
      for (int i4 = 0; i4 < 10; ++i4) {
        const u32x4 xd = *reinterpret_cast<const u32x4*>(&lds[L_XQ + w * QROWS + r * 40 + i4 * 4]);
        a0 = gemv_mfma(wr[4 * i4 + 0], xd[0], msk8, a0);
        a1 = gemv_mfma(wr[4 * i4 + 1], xd[1], msk8, a1);
        a0 = gemv_mfma(wr[4 * i4 + 2], xd[2], msk8, a0);
        a1 = gemv_mfma(wr[4 * i4 + 3], xd[3], msk8, a1);
      }
      a0 += a1;
      if (c < 8) {
#pragma unroll
        for (int g = 0; g < 4; ++g) ldsf[L_QP + w * DH + 8 * (4 * r + g) + c] = a0[g];
      }
    }
    __builtin_amdgcn_s_setprio(0);
    __syncthreads();
    {
      const unsigned ch = min((unsigned)chunk0 + c8, (unsigned)(FF / 8 - 1));
      const char* p = w1 + (size_t)(QROWS * w) * (FF * 2);
      const unsigned lo = r8 * (FF * 2) + ch * 16;
#pragma unroll
      for (int i = 0; i < 20; ++i) { wr[i] = ld_w(p + (size_t)(8 * i) * (FF * 2) + lo); PACE }
      SB
      const char* p2 = w2 + (size_t)(FS * js) * (DM * 2);
#pragma unroll
      for (int g = 0; g < 4; ++g) {
        const unsigned lo2 = min(16u * w + 4 * g + r, (unsigned)(FS - 1)) * (DM * 2) + c * 16;
#pragma unroll
        for (int s = 0; s < 5; ++s) { wr[20 + 5 * g + s] = ld_w(p2 + s * 256 + lo2); PACE }
      }
      SB
      f32x4 a0 = {0.f, 0.f, 0.f, 0.f}, a1 = {0.f, 0.f, 0.f, 0.f};
#pragma unroll
      for (int i4 = 0; i4 < 5; ++i4) {
        const u32x4 xd = *reinterpret_cast<const u32x4*>(&lds[L_X1 + w * QROWS + r8 * 20 + i4 * 4]);
        a0 = gemv_mfma(wr[4 * i4 + 0], xd[0], msk16, a0);
        a1 = gemv_mfma(wr[4 * i4 + 1], xd[1], msk16, a1);
        a0 = gemv_mfma(wr[4 * i4 + 2], xd[2], msk16, a0);
        a1 = gemv_mfma(wr[4 * i4 + 3], xd[3], msk16, a1);
      }
      a0 += a1;
      if ((r >> 1) == (c >> 3)) {
#pragma unroll
        for (int g = 0; g < 4; ++g) ldsf[L_HP + w * 128 + (r >> 1) * 64 + 8 * (4 * (r & 1) + g) + c8] = a0[g];
      }
      __builtin_amdgcn_fence(__ATOMIC_RELEASE, "workgroup", "local");
      if (lane == 0) atomicAdd(&lds[L_CNT + 1], 1u);
      while (__hip_atomic_load(&lds[L_CNT + 1], __ATOMIC_RELAXED, __HIP_MEMORY_SCOPE_WORKGROUP) < (unsigned)NHLP) __builtin_amdgcn_s_sleep(2);
      __builtin_amdgcn_fence(__ATOMIC_ACQUIRE, "workgroup", "local");
      unsigned hd = 0u;
      {
        const unsigned off = FS * js - chunk0 * 8;
        float sm = 0.f;
#pragma unroll
        for (int ww = 0; ww < 2 * NHLP; ++ww) sm += ldsf[L_HP + ww * 64 + ((off + lane) & 63)];
        const float hb = rbf(rbf(sm) + bf1(b1u));
        const float u2 = 1.5957691216057308f * (hb + 0.044715f * hb * hb * hb);
        const unsigned hv = tobf(hb / (1.0f + __expf(-u2)));
        hd = (lane < (unsigned)FS) ? (hv | (hv << 16)) : 0u;
      }
      unsigned hg[4];
#pragma unroll
      for (int g = 0; g < 4; ++g) hg[g] = __builtin_amdgcn_ds_bpermute((int)((16 * w + 4 * g + r) * 4), (int)hd);
#pragma unroll
      for (int s = 0; s < 5; ++s) {
        f32x4 mm = {0.f, 0.f, 0.f, 0.f};
#pragma unroll
        for (int g = 0; g < 4; ++g) mm = gemv_mfma(wr[20 + 5 * g + s], hg[g], msk8, mm);
        if (c < 8) *reinterpret_cast<f32x4*>(&ldsf[L_PP + ((w * 10 + 5 + s) * 32 + r * 8 + c) * 4]) = mm;
      }
    }
    SB
    {
      const char* wop = wo + (size_t)(h * DH + 32 * w) * (DM * 2);
      const unsigned wlo = r * (DM * 2) + c * 16;
#pragma unroll
      for (int u = 0; u < 40; ++u) { wr[u] = ld_w(wop + (u / 5) * (4 * DM * 2) + (u % 5) * 256 + wlo); PACE }
    }
    SB
    __syncthreads();
    unsigned od[8];
    {
      float mv[NSTR], lv[NSTR], av[NSTR];
#pragma unroll
      for (int ww = 0; ww < NSTR; ++ww) {
        const float* rec = &ldsf[L_MG + ww * MG_REC];
        mv[ww] = rec[0]; lv[ww] = rec[1]; av[ww] = rec[4 + 32 * w + (lane & 31)];
      }
      float M = mv[0];
#pragma unroll
      for (int ww = 1; ww < NSTR; ++ww) M = fmaxf(M, mv[ww]);
      float L = 0.f, o = 0.f;
#pragma unroll
      for (int ww = 0; ww < NSTR; ++ww) {
        const float wt = __builtin_amdgcn_exp2f(mv[ww] - M);
        L = fmaf(wt, lv[ww], L);
        o = fmaf(wt, av[ww], o);
      }
      const unsigned ob = tobf(o / L);
      const unsigned odup = ob | (ob << 16);
#pragma unroll
      for (int g = 0; g < 8; ++g) od[g] = __builtin_amdgcn_ds_bpermute((int)((4 * g + r) * 4), (int)odup);
    }
    {
      f32x4 a[5];
#pragma unroll
      for (int s = 0; s < 5; ++s) a[s] = f32x4{0.f, 0.f, 0.f, 0.f};
#pragma unroll
      for (int g = 0; g < 8; ++g) {
#pragma unroll
        for (int s = 0; s < 5; ++s) a[s] = gemv_mfma(wr[5 * g + s], od[g], msk8, a[s]);
      }
      if (c < 8) {
#pragma unroll
        for (int s = 0; s < 5; ++s) *reinterpret_cast<f32x4*>(&ldsf[L_PP + ((w * 10 + s) * 32 + r * 8 + c) * 4]) = a[s];
      }
    }
    __syncthreads();
  }
  if (tid < 320) {
    f32x4 s = {0.f, 0.f, 0.f, 0.f};
#pragma unroll
    for (int ww = 0; ww < NHLP; ++ww) s += *reinterpret_cast<const f32x4*>(&ldsf[L_PP + (ww * 320 + tid) * 4]);
    *reinterpret_cast<f32x4*>(spart + (size_t)bid * 1280 + tid * 4) = s;
  }
}

__global__ void __launch_bounds__(256) final_kernel(const unsigned short* __restrict__ x, const unsigned short* __restrict__ b2,
                                                    const float* __restrict__ spart, unsigned short* __restrict__ out) {
  __shared__ __attribute__((aligned(16))) float red[2][16][64];
  const int b = blockIdx.x / 10, seg = blockIdx.x % 10;
  const int tid = threadIdx.x, cq = tid & 15, g = tid >> 4;
  const size_t base = (size_t)(b * NH + 2 * g) * 1280 + seg * 64 + cq * 4;
  const f32x4 s0 = *reinterpret_cast<const f32x4*>(spart + base);
  const f32x4 s1 = *reinterpret_cast<const f32x4*>(spart + base + 1280);
  const f32x4 m0 = *reinterpret_cast<const f32x4*>(spart + base + 640);
  const f32x4 m1 = *reinterpret_cast<const f32x4*>(spart + base + 1280 + 640);
  const int p = seg * 64 + (tid & 63);
  const int n = (p >> 7) * 128 + 8 * (4 * ((p >> 5) & 3) + (p & 3)) + ((p >> 2) & 7);
  float b2v = bf1(b2[n]);
  float xv = bf1(x[b * DM + n]);
  asm volatile("" : "+v"(b2v), "+v"(xv));
  *reinterpret_cast<f32x4*>(&red[0][g][cq * 4]) = s0 + s1;
  *reinterpret_cast<f32x4*>(&red[1][g][cq * 4]) = m0 + m1;
  __syncthreads();
  if (tid < 64) {
    float s = 0.f, mm = 0.f;
#pragma unroll
    for (int k = 0; k < 16; ++k) { s += red[0][k][tid]; mm += red[1][k][tid]; }
    const float sb = rbf(s);
    const float mb = rbf(rbf(mm) + b2v);
    const float gt = rbf(mb / (1.0f + __expf(-mb)));
    const float res = xv + rbf(sb * gt);
    out[b * DM + n] = tobf(res);
  }
}

}

extern "C" void kernel_launch(void* const* d_in, const int* in_sizes, int n_in, void* d_out, int out_size, void* d_ws,
                              size_t ws_size, hipStream_t stream) {
  const unsigned short* x = (const unsigned short*)d_in[0];
  const char* kc = (const char*)d_in[1];
  const char* vc = (const char*)d_in[2];
  const char* wq = (const char*)d_in[3];
  const char* wo = (const char*)d_in[4];
  const char* w1 = (const char*)d_in[6];
  const unsigned short* b1 = (const unsigned short*)d_in[7];
  const char* w2 = (const char*)d_in[8];
  const unsigned short* b2 = (const unsigned short*)d_in[9];
  float* spart = (float*)d_ws;
  fused_attn_kernel<<<NB * NH, NT, 0, stream>>>(x, kc, vc, wq, wo, w1, b1, w2, spart);
  final_kernel<<<NB * 10, 256, 0, stream>>>(x, b2, spart, (unsigned short*)d_out);
}
```

```cpp
#include <hip/hip_runtime.h>
#include <hip/hip_bf16.h>
#include <stdint.h>

namespace {

constexpr int NB = 8, DM = 640, NH = 32, SK = 4096, DH = 128, FF = 1728;
constexpr int NW = 8;
constexpr int NT = NW * 64;
constexpr int NHLP = 4, NSTR = 4;
constexpr int FS = FF / NH;
constexpr int QROWS = DM / NHLP;

typedef unsigned u32x4 __attribute__((ext_vector_type(4)));
typedef float f32x4 __attribute__((ext_vector_type(4)));
typedef __bf16 bf16x2 __attribute__((ext_vector_type(2)));
typedef __bf16 bf16x8 __attribute__((ext_vector_type(8)));

__device__ __forceinline__ float bf_lo(unsigned u) { return __builtin_bit_cast(float, u << 16); }
__device__ __forceinline__ float bf_hi(unsigned u) { return __builtin_bit_cast(float, u & 0xffff0000u); }
__device__ __forceinline__ float bf1(unsigned short u) { return __builtin_bit_cast(float, (unsigned)u << 16); }
__device__ __forceinline__ float rbf(float x) { return (float)(__bf16)x; }
__device__ __forceinline__ unsigned short tobf(float x) { return __builtin_bit_cast(unsigned short, (__bf16)x); }
__device__ __forceinline__ unsigned pack2(float lo, float hi) { return (unsigned)tobf(lo) | ((unsigned)tobf(hi) << 16); }

__device__ __forceinline__ float dot2(unsigned k, unsigned q, float acc) {
  return __builtin_amdgcn_fdot2_f32_bf16(__builtin_bit_cast(bf16x2, k), __builtin_bit_cast(bf16x2, q), acc, false);
}
template <int CTRL> __device__ __forceinline__ float dpp(float x) {
  return __builtin_bit_cast(float, __builtin_amdgcn_mov_dpp(__builtin_bit_cast(int, x), CTRL, 0xf, 0xf, true));
}
__device__ __forceinline__ float row16_sum(float x) {
  x += dpp<0xB1>(x);
  x += dpp<0x4E>(x);
  x += dpp<0x141>(x);
  x += dpp<0x140>(x);
  return x;
}
__device__ __forceinline__ float xrow_sum(float x) {
  auto s = __builtin_amdgcn_permlane16_swap(__float_as_uint(x), __float_as_uint(x), false, false);
  x = __uint_as_float(s[0]) + __uint_as_float(s[1]);
  auto t = __builtin_amdgcn_permlane32_swap(__float_as_uint(x), __float_as_uint(x), false, false);
  return __uint_as_float(t[0]) + __uint_as_float(t[1]);
}
__device__ __forceinline__ float xrow_max(float x) {
  auto s = __builtin_amdgcn_permlane16_swap(__float_as_uint(x), __float_as_uint(x), false, false);
  x = fmaxf(__uint_as_float(s[0]), __uint_as_float(s[1]));
  auto t = __builtin_amdgcn_permlane32_swap(__float_as_uint(x), __float_as_uint(x), false, false);
  return fmaxf(__uint_as_float(t[0]), __uint_as_float(t[1]));
}
__device__ __forceinline__ float wave_sum(float x) { return xrow_sum(row16_sum(x)); }

__device__ __forceinline__ u32x4 ld_nt(const char* p) { return __builtin_nontemporal_load(reinterpret_cast<const u32x4*>(p)); }
__device__ __forceinline__ u32x4 ld_w(const char* p) { return __builtin_nontemporal_load(reinterpret_cast<const u32x4*>(p)); }

__device__ __forceinline__ f32x4 gemv_mfma(u32x4 a, unsigned xdup, u32x4 msk, f32x4 c) {
  const u32x4 bsel = {xdup & msk[0], xdup & msk[1], xdup & msk[2], xdup & msk[3]};
  return __builtin_amdgcn_mfma_f32_16x16x32_bf16(__builtin_bit_cast(bf16x8, a), __builtin_bit_cast(bf16x8, bsel), c, 0, 0, 0);
}

constexpr int L_XQ = 0;
constexpr int L_X1 = L_XQ + DM;
constexpr int L_QP = L_X1 + DM;
constexpr int L_HP = L_QP + NHLP * DH;
constexpr int L_MG = L_HP + NHLP * 128;
constexpr int MG_REC = DH + 4;
constexpr int L_PP = L_MG + NSTR * MG_REC;
constexpr int L_SC = L_PP + NHLP * 10 * 128;
constexpr int L_CNT = L_SC + NSTR * 1024;
constexpr int L_END = L_CNT + 4;

__global__ void __launch_bounds__(NT, 2) fused_attn_kernel(
    const unsigned short* __restrict__ x, const char* __restrict__ kc, const char* __restrict__ vc,
    const char* __restrict__ wq, const char* __restrict__ wo, const char* __restrict__ w1,
    const unsigned short* __restrict__ b1, const char* __restrict__ w2, float* __restrict__ spart) {
  __shared__ __attribute__((aligned(16))) unsigned lds[L_END];
  float* ldsf = reinterpret_cast<float*>(lds);
  const int tid = threadIdx.x;
  const unsigned lane = tid & 63;
  const int w = __builtin_amdgcn_readfirstlane(tid >> 6);
  const unsigned r = lane >> 4, c = lane & 15;
  const int bid = blockIdx.x;
  const int h = bid & 31, b = bid >> 5;
#define SB __builtin_amdgcn_sched_barrier(0);
#define PACE SB __builtin_amdgcn_s_sleep(16); SB

  if (w >= NHLP) {
    const int j = w - NHLP;
    const char* kb = kc + (size_t)bid * (SK * DH * 2) + j * 4096;
    const char* vb = vc + (size_t)bid * (SK * DH * 2) + j * 4096;
    const unsigned lo16 = lane * 16;
    u32x4 R[8][4];
#define LOAD_P(d, base) _Pragma("unroll") for (int t = 0; t < 4; ++t) R[d][t] = ld_nt((base) + (d) * 16384 + t * 1024 + lo16);
#pragma unroll
    for (int d = 0; d < 8; ++d) { LOAD_P(d, kb) SB }
    u32x4 X[4][4];
#pragma unroll
    for (int d = 0; d < 4; ++d) {
#pragma unroll
      for (int t = 0; t < 4; ++t) X[d][t] = ld_nt(vb + (60 + d) * 16384 + t * 1024 + lo16);
      SB
    }
    __syncthreads();
    u32x4 q;
    {
      const float* qp = &ldsf[L_QP + r * DH + c * 8];
      f32x4 s0 = *reinterpret_cast<const f32x4*>(qp);
      f32x4 s1 = *reinterpret_cast<const f32x4*>(qp + 4);
#pragma unroll
      for (int k = 0; k < 4; ++k) { s0[k] = xrow_sum(s0[k]); s1[k] = xrow_sum(s1[k]); }
      q[0] = pack2(s0[0], s0[1]); q[1] = pack2(s0[2], s0[3]); q[2] = pack2(s1[0], s1[1]); q[3] = pack2(s1[2], s1[3]);
    }
    asm volatile("" : "+v"(q));

    float* scw = &ldsf[L_SC + j * 1024];
    const bool c1 = (c & 1) != 0, c2 = (c & 2) != 0;
    float mx = -INFINITY;
    {
      float* sp = scw + r * 4 + (c & 3);
      const char* nb = kb + 8 * 16384;
      for (int it = 0; it < 8; ++it) {
        if (it == 7) nb = vb;
#pragma unroll
        for (int d = 0; d < 8; ++d) {
          float s_[4];
#pragma unroll
          for (int t = 0; t < 4; ++t) {
            float dd = 0.f;
#pragma unroll
            for (int k = 0; k < 4; ++k) dd = dot2(R[d][t][k], q[k], dd);
            s_[t] = dd;
          }
          asm volatile("" : "+v"(s_[0]), "+v"(s_[1]), "+v"(s_[2]), "+v"(s_[3]));
          LOAD_P(d, nb)
          SB
          const float x0 = (c1 ? s_[1] : s_[0]) + dpp<0xB1>(c1 ? s_[0] : s_[1]);
          const float x1 = (c1 ? s_[3] : s_[2]) + dpp<0xB1>(c1 ? s_[2] : s_[3]);
          float y = (c2 ? x1 : x0) + dpp<0x4E>(c2 ? x0 : x1);
          y += dpp<0x128>(y);
          y += dpp<0x124>(y);
          mx = fmaxf(mx, y);
          if (c < 4) sp[d * 16] = y;
          SB
        }
        sp += 128;
        nb += 8 * 16384;
      }
    }
    constexpr float C2 = 0.08838834764831845f * 1.4426950408889634f;
    float m, l;
    {
      mx = fmaxf(mx, dpp<0xB1>(mx));
      mx = fmaxf(mx, dpp<0x4E>(mx));
      m = xrow_max(mx) * C2;
      __builtin_amdgcn_fence(__ATOMIC_RELEASE, "wavefront");
      __builtin_amdgcn_wave_barrier();
      __builtin_amdgcn_fence(__ATOMIC_ACQUIRE, "wavefront");
      float ls = 0.f;
#pragma unroll
      for (int u = 0; u < 4; ++u) {
        f32x4 v = *reinterpret_cast<const f32x4*>(scw + u * 256 + lane * 4);
#pragma unroll
        for (int k = 0; k < 4; ++k) v[k] = __builtin_amdgcn_exp2f(fmaf(v[k], C2, -m));
        ls += (v[0] + v[1]) + (v[2] + v[3]);
        *reinterpret_cast<f32x4*>(scw + u * 256 + lane * 4) = v;
      }
      l = wave_sum(ls);
      __builtin_amdgcn_fence(__ATOMIC_RELEASE, "wavefront");
      __builtin_amdgcn_wave_barrier();
      __builtin_amdgcn_fence(__ATOMIC_ACQUIRE, "wavefront");
    }
    float acc[8];
#pragma unroll
    for (int k = 0; k < 8; ++k) acc[k] = 0.f;
#define PV_PIECE(RR, d, pp)                                           \
  {                                                                   \
    const f32x4 p_ = *reinterpret_cast<const f32x4*>((pp) + (d) * 16); \
    _Pragma("unroll") for (int t = 0; t < 4; ++t) {                   \
      _Pragma("unroll") for (int k = 0; k < 4; ++k) {                 \
        acc[2 * k] = fmaf(p_[t], bf_lo(RR[d][t][k]), acc[2 * k]);     \
        acc[2 * k + 1] = fmaf(p_[t], bf_hi(RR[d][t][k]), acc[2 * k + 1]); \
      }                                                               \
    }                                                                 \
    asm volatile("" : "+v"(acc[0]), "+v"(acc[1]), "+v"(acc[2]), "+v"(acc[3]), "+v"(acc[4]), "+v"(acc[5]), "+v"(acc[6]), "+v"(acc[7])); \
  }
    {
#pragma unroll
      for (int d = 0; d < 4; ++d) { PV_PIECE(X, d, scw + 60 * 16 + r * 4) SB }
      const float* pp = scw + r * 4;
      const char* nb = vb + 8 * 16384;
      for (int it = 0; it < 6; ++it) {
#pragma unroll
        for (int d = 0; d < 8; ++d) { PV_PIECE(R, d, pp) LOAD_P(d, nb) SB }
        pp += 128;
        nb += 8 * 16384;
      }
#pragma unroll
      for (int d = 0; d < 8; ++d) { PV_PIECE(R, d, pp) if (d < 4) { LOAD_P(d, nb) } SB }
      pp += 128;
#pragma unroll
      for (int d = 0; d < 4; ++d) { PV_PIECE(R, d, pp) SB }
    }
    {
#pragma unroll
      for (int k = 0; k < 8; ++k) acc[k] = xrow_sum(acc[k]);
      float* rec = &ldsf[L_MG + j * MG_REC];
      if (r == 0) {
        *reinterpret_cast<f32x4*>(rec + 4 + c * 8) = f32x4{acc[0], acc[1], acc[2], acc[3]};
        *reinterpret_cast<f32x4*>(rec + 4 + c * 8 + 4) = f32x4{acc[4], acc[5], acc[6], acc[7]};
        if (c == 0) { rec[0] = m; rec[1] = l; }
      }
    }
    __syncthreads();
    __syncthreads();
  } else {
    __builtin_amdgcn_s_setprio(3);
    const int js = (h & 7) * 4 + (h >> 3);
    const unsigned r8 = lane >> 3, c8 = lane & 7;
    const int chunk0 = (FS * js) >> 3;
    const unsigned short* xb = x + b * DM;
    const u32x4 xa = *reinterpret_cast<const u32x4*>(xb + lane * 8);
    const unsigned xt = *reinterpret_cast<const unsigned*>(xb + 512 + lane * 2);
    const unsigned short xo0 = xb[QROWS * w + lane];
    const unsigned short xo1 = xb[QROWS * w + 64 + lane];
    const unsigned short xo2 = xb[QROWS * w + 128 + (lane & 31)];
    const unsigned short b1u = b1[FS * js + min(lane, (unsigned)(FS - 1))];
    SB
    u32x4 wr[40];
    {
      const char* p = wq + (size_t)(QROWS * w) * (NH * DH * 2) + h * (DH * 2);
      const unsigned lo = r * (NH * DH * 2) + c * 16;
#pragma unroll
      for (int i = 0; i < 40; ++i) {
        wr[i] = ld_w(p + (size_t)(4 * i) * (NH * DH * 2) + lo);
        if ((i & 3) == 0) { SB }
      }
    }
    SB
    if (tid == 0) lds[L_CNT + 1] = 0;
    u32x4 msk8, msk16;
#pragma unroll
    for (int k = 0; k < 4; ++k) {
      const unsigned m16 = (c8 == 2u * k) ? 0x0000ffffu : ((c8 == 2u * k + 1) ? 0xffff0000u : 0u);
      msk16[k] = m16;
      msk8[k] = (c < 8) ? m16 : 0u;
    }
    {
      float xv[10];
#pragma unroll
      for (int k = 0; k < 4; ++k) { xv[2 * k] = bf_lo(xa[k]); xv[2 * k + 1] = bf_hi(xa[k]); }
      xv[8] = bf_lo(xt); xv[9] = bf_hi(xt);
      float sm = 0.f;
#pragma unroll
      for (int k = 0; k < 10; ++k) sm += xv[k];
      const float mean = wave_sum(sm) * (1.0f / DM);
      float ss = 0.f;
#pragma unroll
      for (int k = 0; k < 10; ++k) { const float d = xv[k] - mean; ss = fmaf(d, d, ss); }
      const float var = wave_sum(ss) * (1.0f / DM);
      const float mb = rbf(mean);
      const float sd = rbf(sqrtf(rbf(rbf(var) + 1e-5f)));
      const unsigned n0 = tobf(rbf(bf1(xo0) - mb) / sd);
      const unsigned n1 = tobf(rbf(bf1(xo1) - mb) / sd);
      const unsigned n2 = tobf(rbf(bf1(xo2) - mb) / sd);
      const unsigned d0 = n0 | (n0 << 16), d1 = n1 | (n1 << 16), d2 = n2 | (n2 << 16);
      unsigned* xq = &lds[L_XQ + w * QROWS + (lane & 3) * 40 + (lane >> 2)];
      unsigned* x1 = &lds[L_X1 + w * QROWS + (lane & 7) * 20 + (lane >> 3)];
      xq[0] = d0; xq[16] = d1;
      x1[0] = d0; x1[8] = d1;
      if (lane < 32) { xq[32] = d2; x1[16] = d2; }
    }
    __builtin_amdgcn_fence(__ATOMIC_RELEASE, "wavefront");
    __builtin_amdgcn_wave_barrier();
    __builtin_amdgcn_fence(__ATOMIC_ACQUIRE, "wavefront");
    {
      f32x4 a0 = {0.f, 0.f, 0.f, 0.f}, a1 = {0.f, 0.f, 0.f, 0.f};
#pragma unroll
      for (int i4 = 0; i4 < 10; ++i4) {
        const u32x4 xd = *reinterpret_cast<const u32x4*>(&lds[L_XQ + w * QROWS + r * 40 + i4 * 4]);
        a0 = gemv_mfma(wr[4 * i4 + 0], xd[0], msk8, a0);
        a1 = gemv_mfma(wr[4 * i4 + 1], xd[1], msk8, a1);
        a0 = gemv_mfma(wr[4 * i4 + 2], xd[2], msk8, a0);
        a1 = gemv_mfma(wr[4 * i4 + 3], xd[3], msk8, a1);
      }
      a0 += a1;
      if (c < 8) {
#pragma unroll
        for (int g = 0; g < 4; ++g) ldsf[L_QP + w * DH + 8 * (4 * r + g) + c] = a0[g];
      }
    }
    __builtin_amdgcn_s_setprio(0);
    __builtin_amdgcn_s_sleep(75);
    __syncthreads();
    {
      const unsigned ch = min((unsigned)chunk0 + c8, (unsigned)(FF / 8 - 1));
      const char* p = w1 + (size_t)(QROWS * w) * (FF * 2);
      const unsigned lo = r8 * (FF * 2) + ch * 16;
#pragma unroll
      for (int i = 0; i < 20; ++i) { wr[i] = ld_w(p + (size_t)(8 * i) * (FF * 2) + lo); PACE }
      SB
      const char* p2 = w2 + (size_t)(FS * js) * (DM * 2);
#pragma unroll
      for (int g = 0; g < 4; ++g) {
        const unsigned lo2 = min(16u * w + 4 * g + r, (unsigned)(FS - 1)) * (DM * 2) + c * 16;
#pragma unroll
        for (int s = 0; s < 5; ++s) { wr[20 + 5 * g + s] = ld_w(p2 + s * 256 + lo2); PACE }
      }
      SB
      f32x4 a0 = {0.f, 0.f, 0.f, 0.f}, a1 = {0.f, 0.f, 0.f, 0.f};
#pragma unroll
      for (int i4 = 0; i4 < 5; ++i4) {
        const u32x4 xd = *reinterpret_cast<const u32x4*>(&lds[L_X1 + w * QROWS + r8 * 20 + i4 * 4]);
        a0 = gemv_mfma(wr[4 * i4 + 0], xd[0], msk16, a0);
        a1 = gemv_mfma(wr[4 * i4 + 1], xd[1], msk16, a1);
        a0 = gemv_mfma(wr[4 * i4 + 2], xd[2], msk16, a0);
        a1 = gemv_mfma(wr[4 * i4 + 3], xd[3], msk16, a1);
      }
      a0 += a1;
      if ((r >> 1) == (c >> 3)) {
#pragma unroll
        for (int g = 0; g < 4; ++g) ldsf[L_HP + w * 128 + (r >> 1) * 64 + 8 * (4 * (r & 1) + g) + c8] = a0[g];
      }
      __builtin_amdgcn_fence(__ATOMIC_RELEASE, "workgroup", "local");
      if (lane == 0) atomicAdd(&lds[L_CNT + 1], 1u);
      while (__hip_atomic_load(&lds[L_CNT + 1], __ATOMIC_RELAXED, __HIP_MEMORY_SCOPE_WORKGROUP) < (unsigned)NHLP) __builtin_amdgcn_s_sleep(2);
      __builtin_amdgcn_fence(__ATOMIC_ACQUIRE, "workgroup", "local");
      unsigned hd = 0u;
      {
        const unsigned off = FS * js - chunk0 * 8;
        float sm = 0.f;
#pragma unroll
        for (int ww = 0; ww < 2 * NHLP; ++ww) sm += ldsf[L_HP + ww * 64 + ((off + lane) & 63)];
        const float hb = rbf(rbf(sm) + bf1(b1u));
        const float u2 = 1.5957691216057308f * (hb + 0.044715f * hb * hb * hb);
        const unsigned hv = tobf(hb / (1.0f + __expf(-u2)));
        hd = (lane < (unsigned)FS) ? (hv | (hv << 16)) : 0u;
      }
      unsigned hg[4];
#pragma unroll
      for (int g = 0; g < 4; ++g) hg[g] = __builtin_amdgcn_ds_bpermute((int)((16 * w + 4 * g + r) * 4), (int)hd);
#pragma unroll
      for (int s = 0; s < 5; ++s) {
        f32x4 mm = {0.f, 0.f, 0.f, 0.f};
#pragma unroll
        for (int g = 0; g < 4; ++g) mm = gemv_mfma(wr[20 + 5 * g + s], hg[g], msk8, mm);
        if (c < 8) *reinterpret_cast<f32x4*>(&ldsf[L_PP + ((w * 10 + 5 + s) * 32 + r * 8 + c) * 4]) = mm;
      }
    }
    SB
    {
      const char* wop = wo + (size_t)(h * DH + 32 * w) * (DM * 2);
      const unsigned wlo = r * (DM * 2) + c * 16;
#pragma unroll
      for (int u = 0; u < 40; ++u) { wr[u] = ld_w(wop + (u / 5) * (4 * DM * 2) + (u % 5) * 256 + wlo); PACE }
    }
    SB
    __syncthreads();
    unsigned od[8];
    {
      float mv[NSTR], lv[NSTR], av[NSTR];
#pragma unroll
      for (int ww = 0; ww < NSTR; ++ww) {
        const float* rec = &ldsf[L_MG + ww * MG_REC];
        mv[ww] = rec[0]; lv[ww] = rec[1]; av[ww] = rec[4 + 32 * w + (lane & 31)];
      }
      float M = mv[0];
#pragma unroll
      for (int ww = 1; ww < NSTR; ++ww) M = fmaxf(M, mv[ww]);
      float L = 0.f, o = 0.f;
#pragma unroll
      for (int ww = 0; ww < NSTR; ++ww) {
        const float wt = __builtin_amdgcn_exp2f(mv[ww] - M);
        L = fmaf(wt, lv[ww], L);
        o = fmaf(wt, av[ww], o);
      }
      const unsigned ob = tobf(o / L);
      const unsigned odup = ob | (ob << 16);
#pragma unroll
      for (int g = 0; g < 8; ++g) od[g] = __builtin_amdgcn_ds_bpermute((int)((4 * g + r) * 4), (int)odup);
    }
    {
      f32x4 a[5];
#pragma unroll
      for (int s = 0; s < 5; ++s) a[s] = f32x4{0.f, 0.f, 0.f, 0.f};
#pragma unroll
      for (int g = 0; g < 8; ++g) {
#pragma unroll
        for (int s = 0; s < 5; ++s) a[s] = gemv_mfma(wr[5 * g + s], od[g], msk8, a[s]);
      }
      if (c < 8) {
#pragma unroll
        for (int s = 0; s < 5; ++s) *reinterpret_cast<f32x4*>(&ldsf[L_PP + ((w * 10 + s) * 32 + r * 8 + c) * 4]) = a[s];
      }
    }
    __syncthreads();
  }
  if (tid < 320) {
    f32x4 s = {0.f, 0.f, 0.f, 0.f};
#pragma unroll
    for (int ww = 0; ww < NHLP; ++ww) s += *reinterpret_cast<const f32x4*>(&ldsf[L_PP + (ww * 320 + tid) * 4]);
    *reinterpret_cast<f32x4*>(spart + (size_t)bid * 1280 + tid * 4) = s;
  }
}

__global__ void __launch_bounds__(256) final_kernel(const unsigned short* __restrict__ x, const unsigned short* __restrict__ b2,
                                                    const float* __restrict__ spart, unsigned short* __restrict__ out) {
  __shared__ __attribute__((aligned(16))) float red[2][16][64];
  const int b = blockIdx.x / 10, seg = blockIdx.x % 10;
  const int tid = threadIdx.x, cq = tid & 15, g = tid >> 4;
  const size_t base = (size_t)(b * NH + 2 * g) * 1280 + seg * 64 + cq * 4;
  const f32x4 s0 = *reinterpret_cast<const f32x4*>(spart + base);
  const f32x4 s1 = *reinterpret_cast<const f32x4*>(spart + base + 1280);
  const f32x4 m0 = *reinterpret_cast<const f32x4*>(spart + base + 640);
  const f32x4 m1 = *reinterpret_cast<const f32x4*>(spart + base + 1280 + 640);
  const int p = seg * 64 + (tid & 63);
  const int n = (p >> 7) * 128 + 8 * (4 * ((p >> 5) & 3) + (p & 3)) + ((p >> 2) & 7);
  float b2v = bf1(b2[n]);
  float xv = bf1(x[b * DM + n]);
  asm volatile("" : "+v"(b2v), "+v"(xv));
  *reinterpret_cast<f32x4*>(&red[0][g][cq * 4]) = s0 + s1;
  *reinterpret_cast<f32x4*>(&red[1][g][cq * 4]) = m0 + m1;
  __syncthreads();
  if (tid < 64) {
    float s = 0.f, mm = 0.f;
#pragma unroll
    for (int k = 0; k < 16; ++k) { s += red[0][k][tid]; mm += red[1][k][tid]; }
    const float sb = rbf(s);
    const float mb = rbf(rbf(mm) + b2v);
    const float gt = rbf(mb / (1.0f + __expf(-mb)));
    const float res = xv + rbf(sb * gt);
    out[b * DM + n] = tobf(res);
  }
}

}

extern "C" void kernel_launch(void* const* d_in, const int* in_sizes, int n_in, void* d_out, int out_size, void* d_ws,
                              size_t ws_size, hipStream_t stream) {
  const unsigned short* x = (const unsigned short*)d_in[0];
  const char* kc = (const char*)d_in[1];
  const char* vc = (const char*)d_in[2];
  const char* wq = (const char*)d_in[3];
  const char* wo = (const char*)d_in[4];
  const char* w1 = (const char*)d_in[6];
  const unsigned short* b1 = (const unsigned short*)d_in[7];
  const char* w2 = (const char*)d_in[8];
  const unsigned short* b2 = (const unsigned short*)d_in[9];
  float* spart = (float*)d_ws;
  fused_attn_kernel<<<NB * NH, NT, 0, stream>>>(x, kc, vc, wq, wo, w1, b1, w2, spart);
  final_kernel<<<NB * 10, 256, 0, stream>>>(x, b2, spart, (unsigned short*)d_out);
}
```

```cpp
#define WINN 9
#define SLP 20
#define SMOOTH 1
#include <hip/hip_runtime.h>
#include <hip/hip_bf16.h>
#include <stdint.h>

namespace {

constexpr int NB = 8, DM = 640, NH = 32, SK = 4096, DH = 128, FF = 1728;
constexpr int NW = 8;
constexpr int NT = NW * 64;
constexpr int NHLP = 4, NSTR = 4;
constexpr int FS = FF / NH;
constexpr int QROWS = DM / NHLP;

typedef unsigned u32x4 __attribute__((ext_vector_type(4)));
typedef float f32x4 __attribute__((ext_vector_type(4)));
typedef __bf16 bf16x2 __attribute__((ext_vector_type(2)));
typedef __bf16 bf16x8 __attribute__((ext_vector_type(8)));

__device__ __forceinline__ float bf_lo(unsigned u) { return __builtin_bit_cast(float, u << 16); }
__device__ __forceinline__ float bf_hi(unsigned u) { return __builtin_bit_cast(float, u & 0xffff0000u); }
__device__ __forceinline__ float bf1(unsigned short u) { return __builtin_bit_cast(float, (unsigned)u << 16); }
__device__ __forceinline__ float rbf(float x) { return (float)(__bf16)x; }
__device__ __forceinline__ unsigned short tobf(float x) { return __builtin_bit_cast(unsigned short, (__bf16)x); }
__device__ __forceinline__ unsigned pack2(float lo, float hi) { return (unsigned)tobf(lo) | ((unsigned)tobf(hi) << 16); }

__device__ __forceinline__ float dot2(unsigned k, unsigned q, float acc) {
  return __builtin_amdgcn_fdot2_f32_bf16(__builtin_bit_cast(bf16x2, k), __builtin_bit_cast(bf16x2, q), acc, false);
}
template <int CTRL> __device__ __forceinline__ float dpp(float x) {
  return __builtin_bit_cast(float, __builtin_amdgcn_mov_dpp(__builtin_bit_cast(int, x), CTRL, 0xf, 0xf, true));
}
__device__ __forceinline__ float row16_sum(float x) {
  x += dpp<0xB1>(x);
  x += dpp<0x4E>(x);
  x += dpp<0x141>(x);
  x += dpp<0x140>(x);
  return x;
}
__device__ __forceinline__ float xrow_sum(float x) {
  auto s = __builtin_amdgcn_permlane16_swap(__float_as_uint(x), __float_as_uint(x), false, false);
  x = __uint_as_float(s[0]) + __uint_as_float(s[1]);
  auto t = __builtin_amdgcn_permlane32_swap(__float_as_uint(x), __float_as_uint(x), false, false);
  return __uint_as_float(t[0]) + __uint_as_float(t[1]);
}
__device__ __forceinline__ float xrow_max(float x) {
  auto s = __builtin_amdgcn_permlane16_swap(__float_as_uint(x), __float_as_uint(x), false, false);
  x = fmaxf(__uint_as_float(s[0]), __uint_as_float(s[1]));
  auto t = __builtin_amdgcn_permlane32_swap(__float_as_uint(x), __float_as_uint(x), false, false);
  return fmaxf(__uint_as_float(t[0]), __uint_as_float(t[1]));
}
__device__ __forceinline__ float wave_sum(float x) { return xrow_sum(row16_sum(x)); }

__device__ __forceinline__ u32x4 ld_nt(const char* p) { return __builtin_nontemporal_load(reinterpret_cast<const u32x4*>(p)); }
__device__ __forceinline__ u32x4 ld_w(const char* p) { return __builtin_nontemporal_load(reinterpret_cast<const u32x4*>(p)); }

__device__ __forceinline__ f32x4 gemv_mfma(u32x4 a, unsigned xdup, u32x4 msk, f32x4 c) {
  const u32x4 bsel = {xdup & msk[0], xdup & msk[1], xdup & msk[2], xdup & msk[3]};
  return __builtin_amdgcn_mfma_f32_16x16x32_bf16(__builtin_bit_cast(bf16x8, a), __builtin_bit_cast(bf16x8, bsel), c, 0, 0, 0);
}

constexpr int L_XQ = 0;
constexpr int L_X1 = L_XQ + DM;
constexpr int L_QP = L_X1 + DM;
constexpr int L_HP = L_QP + NHLP * DH;
constexpr int L_MG = L_HP + NHLP * 128;
constexpr int MG_REC = DH + 4;
constexpr int L_PP = L_MG + NSTR * MG_REC;
constexpr int L_SC = L_PP + NHLP * 10 * 128;
constexpr int L_CNT = L_SC + NSTR * 1024;
constexpr int L_END = L_CNT + 4;

__global__ void __launch_bounds__(NT, 2) fused_attn_kernel(
    const unsigned short* __restrict__ x, const char* __restrict__ kc, const char* __restrict__ vc,
    const char* __restrict__ wq, const char* __restrict__ wo, const char* __restrict__ w1,
    const unsigned short* __restrict__ b1, const char* __restrict__ w2, float* __restrict__ spart) {
  __shared__ __attribute__((aligned(16))) unsigned lds[L_END];
  float* ldsf = reinterpret_cast<float*>(lds);
  const int tid = threadIdx.x;
  const unsigned lane = tid & 63;
  const int w = __builtin_amdgcn_readfirstlane(tid >> 6);
  const unsigned r = lane >> 4, c = lane & 15;
  const int bid = blockIdx.x;
  const int h = bid & 31, b = bid >> 5;
#define SB __builtin_amdgcn_sched_barrier(0);
#define STR2(x) #x
#define STR(x) STR2(x)
#define WINWAIT asm volatile("s_waitcnt vmcnt(" STR(WINN) ")" ::: "memory");
#define PACE SB __builtin_amdgcn_s_sleep(SLP); SB

  if (w >= NHLP) {
    const int j = w - NHLP;
    const char* kb = kc + (size_t)bid * (SK * DH * 2) + j * 4096;
    const char* vb = vc + (size_t)bid * (SK * DH * 2) + j * 4096;
    const unsigned lo16 = lane * 16;
    u32x4 R[8][4];
#define LOAD_P(d, base) _Pragma("unroll") for (int t = 0; t < 4; ++t) R[d][t] = ld_nt((base) + (d) * 16384 + t * 1024 + lo16);
#define REFILL(d, base) _Pragma("unroll") for (int t = 0; t < 4; ++t) { WINWAIT R[d][t] = ld_nt((base) + (d) * 16384 + t * 1024 + lo16); SB }
#pragma unroll
    for (int d = 0; d < 8; ++d) { LOAD_P(d, kb) SB }
    __syncthreads();
    u32x4 q;
    {
      const float* qp = &ldsf[L_QP + r * DH + c * 8];
      f32x4 s0 = *reinterpret_cast<const f32x4*>(qp);
      f32x4 s1 = *reinterpret_cast<const f32x4*>(qp + 4);
#pragma unroll
      for (int k = 0; k < 4; ++k) { s0[k] = xrow_sum(s0[k]); s1[k] = xrow_sum(s1[k]); }
      q[0] = pack2(s0[0], s0[1]); q[1] = pack2(s0[2], s0[3]); q[2] = pack2(s1[0], s1[1]); q[3] = pack2(s1[2], s1[3]);
    }
    asm volatile("" : "+v"(q));

    float* scw = &ldsf[L_SC + j * 1024];
    const bool c1 = (c & 1) != 0, c2 = (c & 2) != 0;
    float mx = -INFINITY;
    {
      float* sp = scw + r * 4 + (c & 3);
      const char* nb = kb + 8 * 16384;
      for (int it = 0; it < 8; ++it) {
        if (it == 7) nb = vb;
#pragma unroll
        for (int d = 0; d < 8; ++d) {
          float s_[4];
#pragma unroll
          for (int t = 0; t < 4; ++t) {
            float dd = 0.f;
#pragma unroll
            for (int k = 0; k < 4; ++k) dd = dot2(R[d][t][k], q[k], dd);
            s_[t] = dd;
          }
          asm volatile("" : "+v"(s_[0]), "+v"(s_[1]), "+v"(s_[2]), "+v"(s_[3]));
          REFILL(d, nb)
          SB
          const float x0 = (c1 ? s_[1] : s_[0]) + dpp<0xB1>(c1 ? s_[0] : s_[1]);
          const float x1 = (c1 ? s_[3] : s_[2]) + dpp<0xB1>(c1 ? s_[2] : s_[3]);
          float y = (c2 ? x1 : x0) + dpp<0x4E>(c2 ? x0 : x1);
          y += dpp<0x128>(y);
          y += dpp<0x124>(y);
          mx = fmaxf(mx, y);
          if (c < 4) sp[d * 16] = y;
          SB
        }
        sp += 128;
        nb += 8 * 16384;
      }
    }
    constexpr float C2 = 0.08838834764831845f * 1.4426950408889634f;
    float m, l;
    {
      mx = fmaxf(mx, dpp<0xB1>(mx));
      mx = fmaxf(mx, dpp<0x4E>(mx));
      m = xrow_max(mx) * C2;
      __builtin_amdgcn_fence(__ATOMIC_RELEASE, "wavefront");
      __builtin_amdgcn_wave_barrier();
      __builtin_amdgcn_fence(__ATOMIC_ACQUIRE, "wavefront");
      float ls = 0.f;
#pragma unroll
      for (int u = 0; u < 4; ++u) {
        f32x4 v = *reinterpret_cast<const f32x4*>(scw + u * 256 + lane * 4);
#pragma unroll
        for (int k = 0; k < 4; ++k) v[k] = __builtin_amdgcn_exp2f(fmaf(v[k], C2, -m));
        ls += (v[0] + v[1]) + (v[2] + v[3]);
        *reinterpret_cast<f32x4*>(scw + u * 256 + lane * 4) = v;
      }
      l = wave_sum(ls);
      __builtin_amdgcn_fence(__ATOMIC_RELEASE, "wavefront");
      __builtin_amdgcn_wave_barrier();
      __builtin_amdgcn_fence(__ATOMIC_ACQUIRE, "wavefront");
    }
    float acc[8];
#pragma unroll
    for (int k = 0; k < 8; ++k) acc[k] = 0.f;
#define PV_PIECE(d, pp)                                               \
  {                                                                   \
    const f32x4 p_ = *reinterpret_cast<const f32x4*>((pp) + (d) * 16); \
    _Pragma("unroll") for (int t = 0; t < 4; ++t) {                   \
      _Pragma("unroll") for (int k = 0; k < 4; ++k) {                 \
        acc[2 * k] = fmaf(p_[t], bf_lo(R[d][t][k]), acc[2 * k]);      \
        acc[2 * k + 1] = fmaf(p_[t], bf_hi(R[d][t][k]), acc[2 * k + 1]); \
      }                                                               \
    }                                                                 \
    asm volatile("" : "+v"(acc[0]), "+v"(acc[1]), "+v"(acc[2]), "+v"(acc[3]), "+v"(acc[4]), "+v"(acc[5]), "+v"(acc[6]), "+v"(acc[7])); \
  }
    {
      const float* pp = scw + r * 4;
      const char* nb = vb + 8 * 16384;
      for (int it = 0; it < 7; ++it) {
#pragma unroll
        for (int d = 0; d < 8; ++d) { PV_PIECE(d, pp) REFILL(d, nb) SB }
        pp += 128;
        nb += 8 * 16384;
      }
#pragma unroll
      for (int d = 0; d < 8; ++d) { PV_PIECE(d, pp) SB }
    }
    {
#pragma unroll
      for (int k = 0; k < 8; ++k) acc[k] = xrow_sum(acc[k]);
      float* rec = &ldsf[L_MG + j * MG_REC];
      if (r == 0) {
        *reinterpret_cast<f32x4*>(rec + 4 + c * 8) = f32x4{acc[0], acc[1], acc[2], acc[3]};
        *reinterpret_cast<f32x4*>(rec + 4 + c * 8 + 4) = f32x4{acc[4], acc[5], acc[6], acc[7]};
        if (c == 0) { rec[0] = m; rec[1] = l; }
      }
    }
    __syncthreads();
    __syncthreads();
  } else {
    __builtin_amdgcn_s_setprio(3);
    const int js = (h & 7) * 4 + (h >> 3);
    const unsigned r8 = lane >> 3, c8 = lane & 7;
    const int chunk0 = (FS * js) >> 3;
    const unsigned short* xb = x + b * DM;
    const u32x4 xa = *reinterpret_cast<const u32x4*>(xb + lane * 8);
    const unsigned xt = *reinterpret_cast<const unsigned*>(xb + 512 + lane * 2);
    const unsigned short xo0 = xb[QROWS * w + lane];
    const unsigned short xo1 = xb[QROWS * w + 64 + lane];
    const unsigned short xo2 = xb[QROWS * w + 128 + (lane & 31)];
    const unsigned short b1u = b1[FS * js + min(lane, (unsigned)(FS - 1))];
    SB
    u32x4 wr[40];
    {
      const char* p = wq + (size_t)(QROWS * w) * (NH * DH * 2) + h * (DH * 2);
      const unsigned lo = r * (NH * DH * 2) + c * 16;
#pragma unroll
      for (int i = 0; i < 40; ++i) {
        wr[i] = ld_w(p + (size_t)(4 * i) * (NH * DH * 2) + lo);
        if ((i & 3) == 0) { SB }
      }
    }
    SB
    if (tid == 0) lds[L_CNT + 1] = 0;
    u32x4 msk8, msk16;
#pragma unroll
    for (int k = 0; k < 4; ++k) {
      const unsigned m16 = (c8 == 2u * k) ? 0x0000ffffu : ((c8 == 2u * k + 1) ? 0xffff0000u : 0u);
      msk16[k] = m16;
      msk8[k] = (c < 8) ? m16 : 0u;
    }
    {
      float xv[10];
#pragma unroll
      for (int k = 0; k < 4; ++k) { xv[2 * k] = bf_lo(xa[k]); xv[2 * k + 1] = bf_hi(xa[k]); }
      xv[8] = bf_lo(xt); xv[9] = bf_hi(xt);
      float sm = 0.f;
#pragma unroll
      for (int k = 0; k < 10; ++k) sm += xv[k];
      const float mean = wave_sum(sm) * (1.0f / DM);
      float ss = 0.f;
#pragma unroll
      for (int k = 0; k < 10; ++k) { const float d = xv[k] - mean; ss = fmaf(d, d, ss); }
      const float var = wave_sum(ss) * (1.0f / DM);
      const float mb = rbf(mean);
      const float sd = rbf(sqrtf(rbf(rbf(var) + 1e-5f)));
      const unsigned n0 = tobf(rbf(bf1(xo0) - mb) / sd);
      const unsigned n1 = tobf(rbf(bf1(xo1) - mb) / sd);
      const unsigned n2 = tobf(rbf(bf1(xo2) - mb) / sd);
      const unsigned d0 = n0 | (n0 << 16), d1 = n1 | (n1 << 16), d2 = n2 | (n2 << 16);
      unsigned* xq = &lds[L_XQ + w * QROWS + (lane & 3) * 40 + (lane >> 2)];
      unsigned* x1 = &lds[L_X1 + w * QROWS + (lane & 7) * 20 + (lane >> 3)];
      xq[0] = d0; xq[16] = d1;
      x1[0] = d0; x1[8] = d1;
      if (lane < 32) { xq[32] = d2; x1[16] = d2; }
    }
    __builtin_amdgcn_fence(__ATOMIC_RELEASE, "wavefront");
    __builtin_amdgcn_wave_barrier();
    __builtin_amdgcn_fence(__ATOMIC_ACQUIRE, "wavefront");
    {
      f32x4 a0 = {0.f, 0.f, 0.f, 0.f}, a1 = {0.f, 0.f, 0.f, 0.f};
#pragma unroll
      for (int i4 = 0; i4 < 10; ++i4) {
        const u32x4 xd = *reinterpret_cast<const u32x4*>(&lds[L_XQ + w * QROWS + r * 40 + i4 * 4]);
        a0 = gemv_mfma(wr[4 * i4 + 0], xd[0], msk8, a0);
        a1 = gemv_mfma(wr[4 * i4 + 1], xd[1], msk8, a1);
        a0 = gemv_mfma(wr[4 * i4 + 2], xd[2], msk8, a0);
        a1 = gemv_mfma(wr[4 * i4 + 3], xd[3], msk8, a1);
      }
      a0 += a1;
      if (c < 8) {
#pragma unroll
        for (int g = 0; g < 4; ++g) ldsf[L_QP + w * DH + 8 * (4 * r + g) + c] = a0[g];
      }
    }
    __builtin_amdgcn_s_setprio(0);
    __syncthreads();
    {
      const unsigned ch = min((unsigned)chunk0 + c8, (unsigned)(FF / 8 - 1));
      const char* p = w1 + (size_t)(QROWS * w) * (FF * 2);
      const unsigned lo = r8 * (FF * 2) + ch * 16;
#pragma unroll
      for (int i = 0; i < 20; ++i) { wr[i] = ld_w(p + (size_t)(8 * i) * (FF * 2) + lo); PACE }
      SB
      const char* p2 = w2 + (size_t)(FS * js) * (DM * 2);
#pragma unroll
      for (int g = 0; g < 4; ++g) {
        const unsigned lo2 = min(16u * w + 4 * g + r, (unsigned)(FS - 1)) * (DM * 2) + c * 16;
#pragma unroll
        for (int s = 0; s < 5; ++s) { wr[20 + 5 * g + s] = ld_w(p2 + s * 256 + lo2); PACE }
      }
      SB
      f32x4 a0 = {0.f, 0.f, 0.f, 0.f}, a1 = {0.f, 0.f, 0.f, 0.f};
#pragma unroll
      for (int i4 = 0; i4 < 5; ++i4) {
        const u32x4 xd = *reinterpret_cast<const u32x4*>(&lds[L_X1 + w * QROWS + r8 * 20 + i4 * 4]);
        a0 = gemv_mfma(wr[4 * i4 + 0], xd[0], msk16, a0);
        a1 = gemv_mfma(wr[4 * i4 + 1], xd[1], msk16, a1);
        a0 = gemv_mfma(wr[4 * i4 + 2], xd[2], msk16, a0);
        a1 = gemv_mfma(wr[4 * i4 + 3], xd[3], msk16, a1);
      }
      a0 += a1;
      if ((r >> 1) == (c >> 3)) {
#pragma unroll
        for (int g = 0; g < 4; ++g) ldsf[L_HP + w * 128 + (r >> 1) * 64 + 8 * (4 * (r & 1) + g) + c8] = a0[g];
      }
      __builtin_amdgcn_fence(__ATOMIC_RELEASE, "workgroup", "local");
      if (lane == 0) atomicAdd(&lds[L_CNT + 1], 1u);
      while (__hip_atomic_load(&lds[L_CNT + 1], __ATOMIC_RELAXED, __HIP_MEMORY_SCOPE_WORKGROUP) < (unsigned)NHLP) __builtin_amdgcn_s_sleep(2);
      __builtin_amdgcn_fence(__ATOMIC_ACQUIRE, "workgroup", "local");
      unsigned hd = 0u;
      {
        const unsigned off = FS * js - chunk0 * 8;
        float sm = 0.f;
#pragma unroll
        for (int ww = 0; ww < 2 * NHLP; ++ww) sm += ldsf[L_HP + ww * 64 + ((off + lane) & 63)];
        const float hb = rbf(rbf(sm) + bf1(b1u));
        const float u2 = 1.5957691216057308f * (hb + 0.044715f * hb * hb * hb);
        const unsigned hv = tobf(hb / (1.0f + __expf(-u2)));
        hd = (lane < (unsigned)FS) ? (hv | (hv << 16)) : 0u;
      }
      unsigned hg[4];
#pragma unroll
      for (int g = 0; g < 4; ++g) hg[g] = __builtin_amdgcn_ds_bpermute((int)((16 * w + 4 * g + r) * 4), (int)hd);
#pragma unroll
      for (int s = 0; s < 5; ++s) {
        f32x4 mm = {0.f, 0.f, 0.f, 0.f};
#pragma unroll
        for (int g = 0; g < 4; ++g) mm = gemv_mfma(wr[20 + 5 * g + s], hg[g], msk8, mm);
        if (c < 8) *reinterpret_cast<f32x4*>(&ldsf[L_PP + ((w * 10 + 5 + s) * 32 + r * 8 + c) * 4]) = mm;
      }
    }
    SB
    {
      const char* wop = wo + (size_t)(h * DH + 32 * w) * (DM * 2);
      const unsigned wlo = r * (DM * 2) + c * 16;
#pragma unroll
      for (int u = 0; u < 40; ++u) { wr[u] = ld_w(wop + (u / 5) * (4 * DM * 2) + (u % 5) * 256 + wlo); PACE }
    }
    SB
    __syncthreads();
    unsigned od[8];
    {
      float mv[NSTR], lv[NSTR], av[NSTR];
#pragma unroll
      for (int ww = 0; ww < NSTR; ++ww) {
        const float* rec = &ldsf[L_MG + ww * MG_REC];
        mv[ww] = rec[0]; lv[ww] = rec[1]; av[ww] = rec[4 + 32 * w + (lane & 31)];
      }
      float M = mv[0];
#pragma unroll
      for (int ww = 1; ww < NSTR; ++ww) M = fmaxf(M, mv[ww]);
      float L = 0.f, o = 0.f;
#pragma unroll
      for (int ww = 0; ww < NSTR; ++ww) {
        const float wt = __builtin_amdgcn_exp2f(mv[ww] - M);
        L = fmaf(wt, lv[ww], L);
        o = fmaf(wt, av[ww], o);
      }
      const unsigned ob = tobf(o / L);
      const unsigned odup = ob | (ob << 16);
#pragma unroll
      for (int g = 0; g < 8; ++g) od[g] = __builtin_amdgcn_ds_bpermute((int)((4 * g + r) * 4), (int)odup);
    }
    {
      f32x4 a[5];
#pragma unroll
      for (int s = 0; s < 5; ++s) a[s] = f32x4{0.f, 0.f, 0.f, 0.f};
#pragma unroll
      for (int g = 0; g < 8; ++g) {
#pragma unroll
        for (int s = 0; s < 5; ++s) a[s] = gemv_mfma(wr[5 * g + s], od[g], msk8, a[s]);
      }
      if (c < 8) {
#pragma unroll
        for (int s = 0; s < 5; ++s) *reinterpret_cast<f32x4*>(&ldsf[L_PP + ((w * 10 + s) * 32 + r * 8 + c) * 4]) = a[s];
      }
    }
    __syncthreads();
  }
  if (tid < 320) {
    f32x4 s = {0.f, 0.f, 0.f, 0.f};
#pragma unroll
    for (int ww = 0; ww < NHLP; ++ww) s += *reinterpret_cast<const f32x4*>(&ldsf[L_PP + (ww * 320 + tid) * 4]);
    *reinterpret_cast<f32x4*>(spart + (size_t)bid * 1280 + tid * 4) = s;
  }
}

__global__ void __launch_bounds__(256) final_kernel(const unsigned short* __restrict__ x, const unsigned short* __restrict__ b2,
                                                    const float* __restrict__ spart, unsigned short* __restrict__ out) {
  __shared__ __attribute__((aligned(16))) float red[2][16][64];
  const int b = blockIdx.x / 10, seg = blockIdx.x % 10;
  const int tid = threadIdx.x, cq = tid & 15, g = tid >> 4;
  const size_t base = (size_t)(b * NH + 2 * g) * 1280 + seg * 64 + cq * 4;
  const f32x4 s0 = *reinterpret_cast<const f32x4*>(spart + base);
  const f32x4 s1 = *reinterpret_cast<const f32x4*>(spart + base + 1280);
  const f32x4 m0 = *reinterpret_cast<const f32x4*>(spart + base + 640);
  const f32x4 m1 = *reinterpret_cast<const f32x4*>(spart + base + 1280 + 640);
  const int p = seg * 64 + (tid & 63);
  const int n = (p >> 7) * 128 + 8 * (4 * ((p >> 5) & 3) + (p & 3)) + ((p >> 2) & 7);
  float b2v = bf1(b2[n]);
  float xv = bf1(x[b * DM + n]);
  asm volatile("" : "+v"(b2v), "+v"(xv));
  *reinterpret_cast<f32x4*>(&red[0][g][cq * 4]) = s0 + s1;
  *reinterpret_cast<f32x4*>(&red[1][g][cq * 4]) = m0 + m1;
  __syncthreads();
  if (tid < 64) {
    float s = 0.f, mm = 0.f;
#pragma unroll
    for (int k = 0; k < 16; ++k) { s += red[0][k][tid]; mm += red[1][k][tid]; }
    const float sb = rbf(s);
    const float mb = rbf(rbf(mm) + b2v);
    const float gt = rbf(mb / (1.0f + __expf(-mb)));
    const float res = xv + rbf(sb * gt);
    out[b * DM + n] = tobf(res);
  }
}

}

extern "C" void kernel_launch(void* const* d_in, const int* in_sizes, int n_in, void* d_out, int out_size, void* d_ws,
                              size_t ws_size, hipStream_t stream) {
  const unsigned short* x = (const unsigned short*)d_in[0];
  const char* kc = (const char*)d_in[1];
  const char* vc = (const char*)d_in[2];
  const char* wq = (const char*)d_in[3];
  const char* wo = (const char*)d_in[4];
  const char* w1 = (const char*)d_in[6];
  const unsigned short* b1 = (const unsigned short*)d_in[7];
  const char* w2 = (const char*)d_in[8];
  const unsigned short* b2 = (const unsigned short*)d_in[9];
  float* spart = (float*)d_ws;
  fused_attn_kernel<<<NB * NH, NT, 0, stream>>>(x, kc, vc, wq, wo, w1, b1, w2, spart);
  final_kernel<<<NB * 10, 256, 0, stream>>>(x, b2, spart, (unsigned short*)d_out);
}
```

```cpp
#include <hip/hip_runtime.h>
#include <hip/hip_bf16.h>
#include <stdint.h>

namespace {

constexpr int NB = 8, DM = 640, NH = 32, SK = 4096, DH = 128, FF = 1728;
constexpr int NW = 8;
constexpr int NT = NW * 64;
constexpr int NHLP = 4, NSTR = 4;
constexpr int FS = FF / NH;
constexpr int QROWS = DM / NHLP;

typedef unsigned u32x4 __attribute__((ext_vector_type(4)));
typedef float f32x4 __attribute__((ext_vector_type(4)));
typedef __bf16 bf16x2 __attribute__((ext_vector_type(2)));
typedef __bf16 bf16x8 __attribute__((ext_vector_type(8)));

__device__ __forceinline__ float bf_lo(unsigned u) { return __builtin_bit_cast(float, u << 16); }
__device__ __forceinline__ float bf_hi(unsigned u) { return __builtin_bit_cast(float, u & 0xffff0000u); }
__device__ __forceinline__ float bf1(unsigned short u) { return __builtin_bit_cast(float, (unsigned)u << 16); }
__device__ __forceinline__ float rbf(float x) { return (float)(__bf16)x; }
__device__ __forceinline__ unsigned short tobf(float x) { return __builtin_bit_cast(unsigned short, (__bf16)x); }
__device__ __forceinline__ unsigned pack2(float lo, float hi) { return (unsigned)tobf(lo) | ((unsigned)tobf(hi) << 16); }

__device__ __forceinline__ float dot2(unsigned k, unsigned q, float acc) {
  return __builtin_amdgcn_fdot2_f32_bf16(__builtin_bit_cast(bf16x2, k), __builtin_bit_cast(bf16x2, q), acc, false);
}
template <int CTRL> __device__ __forceinline__ float dpp(float x) {
  return __builtin_bit_cast(float, __builtin_amdgcn_mov_dpp(__builtin_bit_cast(int, x), CTRL, 0xf, 0xf, true));
}
__device__ __forceinline__ float row16_sum(float x) {
  x += dpp<0xB1>(x);
  x += dpp<0x4E>(x);
  x += dpp<0x141>(x);
  x += dpp<0x140>(x);
  return x;
}
__device__ __forceinline__ float xrow_sum(float x) {
  auto s = __builtin_amdgcn_permlane16_swap(__float_as_uint(x), __float_as_uint(x), false, false);
  x = __uint_as_float(s[0]) + __uint_as_float(s[1]);
  auto t = __builtin_amdgcn_permlane32_swap(__float_as_uint(x), __float_as_uint(x), false, false);
  return __uint_as_float(t[0]) + __uint_as_float(t[1]);
}
__device__ __forceinline__ float xrow_max(float x) {
  auto s = __builtin_amdgcn_permlane16_swap(__float_as_uint(x), __float_as_uint(x), false, false);
  x = fmaxf(__uint_as_float(s[0]), __uint_as_float(s[1]));
  auto t = __builtin_amdgcn_permlane32_swap(__float_as_uint(x), __float_as_uint(x), false, false);
  return fmaxf(__uint_as_float(t[0]), __uint_as_float(t[1]));
}
__device__ __forceinline__ float wave_sum(float x) { return xrow_sum(row16_sum(x)); }

__device__ __forceinline__ u32x4 ld_nt(const char* p) { return __builtin_nontemporal_load(reinterpret_cast<const u32x4*>(p)); }
__device__ __forceinline__ u32x4 ld_w(const char* p) { return __builtin_nontemporal_load(reinterpret_cast<const u32x4*>(p)); }

__device__ __forceinline__ f32x4 gemv_mfma(u32x4 a, unsigned xdup, u32x4 msk, f32x4 c) {
  const u32x4 bsel = {xdup & msk[0], xdup & msk[1], xdup & msk[2], xdup & msk[3]};
  return __builtin_amdgcn_mfma_f32_16x16x32_bf16(__builtin_bit_cast(bf16x8, a), __builtin_bit_cast(bf16x8, bsel), c, 0, 0, 0);
}

constexpr int L_XQ = 0;
constexpr int L_X1 = L_XQ + DM;
constexpr int L_QP = L_X1 + DM;
constexpr int L_HP = L_QP + NHLP * DH;
constexpr int L_MG = L_HP + NHLP * 128;
constexpr int MG_REC = DH + 4;
constexpr int L_PP = L_MG + NSTR * MG_REC;
constexpr int L_SC = L_PP + NHLP * 10 * 128;
constexpr int L_CNT = L_SC + NSTR * 1024;
constexpr int L_END = L_CNT + 4;

__global__ void __launch_bounds__(NT, 2) fused_attn_kernel(
    const unsigned short* __restrict__ x, const char* __restrict__ kc, const char* __restrict__ vc,
    const char* __restrict__ wq, const char* __restrict__ wo, const char* __restrict__ w1,
    const unsigned short* __restrict__ b1, const char* __restrict__ w2, float* __restrict__ spart) {
  __shared__ __attribute__((aligned(16))) unsigned lds[L_END];
  float* ldsf = reinterpret_cast<float*>(lds);
  const int tid = threadIdx.x;
  const unsigned lane = tid & 63;
  const int w = __builtin_amdgcn_readfirstlane(tid >> 6);
  const unsigned r = lane >> 4, c = lane & 15;
  const int bid = blockIdx.x;
  const int h = bid & 31, b = bid >> 5;
#define SB __builtin_amdgcn_sched_barrier(0);
#define PACE SB __builtin_amdgcn_s_sleep(20); SB

  if (w >= NHLP) {
    const int j = w - NHLP;
    const char* kb = kc + (size_t)bid * (SK * DH * 2) + j * 4096;
    const char* vb = vc + (size_t)bid * (SK * DH * 2) + j * 4096;
    const unsigned lo16 = lane * 16;
    u32x4 R[8][4];
#define LOAD_P(d, base) _Pragma("unroll") for (int t = 0; t < 4; ++t) R[d][t] = ld_nt((base) + (d) * 16384 + t * 1024 + lo16);
#define REFILL(d, base, lim) _Pragma("unroll") for (int t = 0; t < 4; ++t) {                                      \
      asm volatile("s_cmp_eq_u32 %0, 0\n\ts_cbranch_scc1 1f\n\ts_waitcnt vmcnt(7)\n1:" : : "s"(lim) : "scc", "memory"); \
      R[d][t] = ld_nt((base) + (d) * 16384 + t * 1024 + lo16); SB }
#pragma unroll
    for (int d = 0; d < 8; ++d) { LOAD_P(d, kb) SB }
    __syncthreads();
    u32x4 q;
    {
      const float* qp = &ldsf[L_QP + r * DH + c * 8];
      f32x4 s0 = *reinterpret_cast<const f32x4*>(qp);
      f32x4 s1 = *reinterpret_cast<const f32x4*>(qp + 4);
#pragma unroll
      for (int k = 0; k < 4; ++k) { s0[k] = xrow_sum(s0[k]); s1[k] = xrow_sum(s1[k]); }
      q[0] = pack2(s0[0], s0[1]); q[1] = pack2(s0[2], s0[3]); q[2] = pack2(s1[0], s1[1]); q[3] = pack2(s1[2], s1[3]);
    }
    asm volatile("" : "+v"(q));

    float* scw = &ldsf[L_SC + j * 1024];
    const bool c1 = (c & 1) != 0, c2 = (c & 2) != 0;
    float mx = -INFINITY;
    {
      float* sp = scw + r * 4 + (c & 3);
      const char* nb = kb + 8 * 16384;
      for (int it = 0; it < 8; ++it) {
        if (it == 7) nb = vb;
#pragma unroll
        for (int d = 0; d < 8; ++d) {
          float s_[4];
#pragma unroll
          for (int t = 0; t < 4; ++t) {
            float dd = 0.f;
#pragma unroll
            for (int k = 0; k < 4; ++k) dd = dot2(R[d][t][k], q[k], dd);
            s_[t] = dd;
          }
          asm volatile("" : "+v"(s_[0]), "+v"(s_[1]), "+v"(s_[2]), "+v"(s_[3]));
          REFILL(d, nb, 1)
          SB
          const float x0 = (c1 ? s_[1] : s_[0]) + dpp<0xB1>(c1 ? s_[0] : s_[1]);
          const float x1 = (c1 ? s_[3] : s_[2]) + dpp<0xB1>(c1 ? s_[2] : s_[3]);
          float y = (c2 ? x1 : x0) + dpp<0x4E>(c2 ? x0 : x1);
          y += dpp<0x128>(y);
          y += dpp<0x124>(y);
          mx = fmaxf(mx, y);
          if (c < 4) sp[d * 16] = y;
          SB
        }
        sp += 128;
        nb += 8 * 16384;
      }
    }
    constexpr float C2 = 0.08838834764831845f * 1.4426950408889634f;
    float m, l;
    {
      mx = fmaxf(mx, dpp<0xB1>(mx));
      mx = fmaxf(mx, dpp<0x4E>(mx));
      m = xrow_max(mx) * C2;
      __builtin_amdgcn_fence(__ATOMIC_RELEASE, "wavefront");
      __builtin_amdgcn_wave_barrier();
      __builtin_amdgcn_fence(__ATOMIC_ACQUIRE, "wavefront");
      float ls = 0.f;
#pragma unroll
      for (int u = 0; u < 4; ++u) {
        f32x4 v = *reinterpret_cast<const f32x4*>(scw + u * 256 + lane * 4);
#pragma unroll
        for (int k = 0; k < 4; ++k) v[k] = __builtin_amdgcn_exp2f(fmaf(v[k], C2, -m));
        ls += (v[0] + v[1]) + (v[2] + v[3]);
        *reinterpret_cast<f32x4*>(scw + u * 256 + lane * 4) = v;
      }
      l = wave_sum(ls);
      __builtin_amdgcn_fence(__ATOMIC_RELEASE, "wavefront");
      __builtin_amdgcn_wave_barrier();
      __builtin_amdgcn_fence(__ATOMIC_ACQUIRE, "wavefront");
    }
    float acc[8];
#pragma unroll
    for (int k = 0; k < 8; ++k) acc[k] = 0.f;
#define PV_PIECE(d, pp)                                               \
  {                                                                   \
    const f32x4 p_ = *reinterpret_cast<const f32x4*>((pp) + (d) * 16); \
    _Pragma("unroll") for (int t = 0; t < 4; ++t) {                   \
      _Pragma("unroll") for (int k = 0; k < 4; ++k) {                 \
        acc[2 * k] = fmaf(p_[t], bf_lo(R[d][t][k]), acc[2 * k]);      \
        acc[2 * k + 1] = fmaf(p_[t], bf_hi(R[d][t][k]), acc[2 * k + 1]); \
      }                                                               \
    }                                                                 \
    asm volatile("" : "+v"(acc[0]), "+v"(acc[1]), "+v"(acc[2]), "+v"(acc[3]), "+v"(acc[4]), "+v"(acc[5]), "+v"(acc[6]), "+v"(acc[7])); \
  }
    {
      const float* pp = scw + r * 4;
      const char* nb = vb + 8 * 16384;
      for (int it = 0; it < 7; ++it) {
#pragma unroll
        for (int d = 0; d < 8; ++d) { PV_PIECE(d, pp) REFILL(d, nb, 6 - it) SB }
        pp += 128;
        nb += 8 * 16384;
      }
#pragma unroll
      for (int d = 0; d < 8; ++d) { PV_PIECE(d, pp) SB }
    }
    {
#pragma unroll
      for (int k = 0; k < 8; ++k) acc[k] = xrow_sum(acc[k]);
      float* rec = &ldsf[L_MG + j * MG_REC];
      if (r == 0) {
        *reinterpret_cast<f32x4*>(rec + 4 + c * 8) = f32x4{acc[0], acc[1], acc[2], acc[3]};
        *reinterpret_cast<f32x4*>(rec + 4 + c * 8 + 4) = f32x4{acc[4], acc[5], acc[6], acc[7]};
        if (c == 0) { rec[0] = m; rec[1] = l; }
      }
    }
    __syncthreads();
    __syncthreads();
  } else {
    __builtin_amdgcn_s_setprio(3);
    const int js = (h & 7) * 4 + (h >> 3);
    const unsigned r8 = lane >> 3, c8 = lane & 7;
    const int chunk0 = (FS * js) >> 3;
    const unsigned short* xb = x + b * DM;
    const u32x4 xa = *reinterpret_cast<const u32x4*>(xb + lane * 8);
    const unsigned xt = *reinterpret_cast<const unsigned*>(xb + 512 + lane * 2);
    const unsigned short xo0 = xb[QROWS * w + lane];
    const unsigned short xo1 = xb[QROWS * w + 64 + lane];
    const unsigned short xo2 = xb[QROWS * w + 128 + (lane & 31)];
    const unsigned short b1u = b1[FS * js + min(lane, (unsigned)(FS - 1))];
    SB
    u32x4 wr[40];
    {
      const char* p = wq + (size_t)(QROWS * w) * (NH * DH * 2) + h * (DH * 2);
      const unsigned lo = r * (NH * DH * 2) + c * 16;
#pragma unroll
      for (int i = 0; i < 40; ++i) {
        wr[i] = ld_w(p + (size_t)(4 * i) * (NH * DH * 2) + lo);
        if ((i & 3) == 0) { SB }
      }
    }
    SB
    if (tid == 0) lds[L_CNT + 1] = 0;
    u32x4 msk8, msk16;
#pragma unroll
    for (int k = 0; k < 4; ++k) {
      const unsigned m16 = (c8 == 2u * k) ? 0x0000ffffu : ((c8 == 2u * k + 1) ? 0xffff0000u : 0u);
      msk16[k] = m16;
      msk8[k] = (c < 8) ? m16 : 0u;
    }
    {
      float xv[10];
#pragma unroll
      for (int k = 0; k < 4; ++k) { xv[2 * k] = bf_lo(xa[k]); xv[2 * k + 1] = bf_hi(xa[k]); }
      xv[8] = bf_lo(xt); xv[9] = bf_hi(xt);
      float sm = 0.f;
#pragma unroll
      for (int k = 0; k < 10; ++k) sm += xv[k];
      const float mean = wave_sum(sm) * (1.0f / DM);
      float ss = 0.f;
#pragma unroll
      for (int k = 0; k < 10; ++k) { const float d = xv[k] - mean; ss = fmaf(d, d, ss); }
      const float var = wave_sum(ss) * (1.0f / DM);
      const float mb = rbf(mean);
      const float sd = rbf(sqrtf(rbf(rbf(var) + 1e-5f)));
      const unsigned n0 = tobf(rbf(bf1(xo0) - mb) / sd);
      const unsigned n1 = tobf(rbf(bf1(xo1) - mb) / sd);
      const unsigned n2 = tobf(rbf(bf1(xo2) - mb) / sd);
      const unsigned d0 = n0 | (n0 << 16), d1 = n1 | (n1 << 16), d2 = n2 | (n2 << 16);
      unsigned* xq = &lds[L_XQ + w * QROWS + (lane & 3) * 40 + (lane >> 2)];
      unsigned* x1 = &lds[L_X1 + w * QROWS + (lane & 7) * 20 + (lane >> 3)];
      xq[0] = d0; xq[16] = d1;
      x1[0] = d0; x1[8] = d1;
      if (lane < 32) { xq[32] = d2; x1[16] = d2; }
    }
    __builtin_amdgcn_fence(__ATOMIC_RELEASE, "wavefront");
    __builtin_amdgcn_wave_barrier();
    __builtin_amdgcn_fence(__ATOMIC_ACQUIRE, "wavefront");
    {
      f32x4 a0 = {0.f, 0.f, 0.f, 0.f}, a1 = {0.f, 0.f, 0.f, 0.f};
#pragma unroll
      for (int i4 = 0; i4 < 10; ++i4) {
        const u32x4 xd = *reinterpret_cast<const u32x4*>(&lds[L_XQ + w * QROWS + r * 40 + i4 * 4]);
        a0 = gemv_mfma(wr[4 * i4 + 0], xd[0], msk8, a0);
        a1 = gemv_mfma(wr[4 * i4 + 1], xd[1], msk8, a1);
        a0 = gemv_mfma(wr[4 * i4 + 2], xd[2], msk8, a0);
        a1 = gemv_mfma(wr[4 * i4 + 3], xd[3], msk8, a1);
      }
      a0 += a1;
      if (c < 8) {
#pragma unroll
        for (int g = 0; g < 4; ++g) ldsf[L_QP + w * DH + 8 * (4 * r + g) + c] = a0[g];
      }
    }
    __builtin_amdgcn_s_setprio(0);
    __syncthreads();
    {
      const unsigned ch = min((unsigned)chunk0 + c8, (unsigned)(FF / 8 - 1));
      const char* p = w1 + (size_t)(QROWS * w) * (FF * 2);
      const unsigned lo = r8 * (FF * 2) + ch * 16;
#pragma unroll
      for (int i = 0; i < 20; ++i) { wr[i] = ld_w(p + (size_t)(8 * i) * (FF * 2) + lo); PACE }
      SB
      const char* p2 = w2 + (size_t)(FS * js) * (DM * 2);
#pragma unroll
      for (int g = 0; g < 4; ++g) {
        const unsigned lo2 = min(16u * w + 4 * g + r, (unsigned)(FS - 1)) * (DM * 2) + c * 16;
#pragma unroll
        for (int s = 0; s < 5; ++s) { wr[20 + 5 * g + s] = ld_w(p2 + s * 256 + lo2); PACE }
      }
      SB
      f32x4 a0 = {0.f, 0.f, 0.f, 0.f}, a1 = {0.f, 0.f, 0.f, 0.f};
#pragma unroll
      for (int i4 = 0; i4 < 5; ++i4) {
        const u32x4 xd = *reinterpret_cast<const u32x4*>(&lds[L_X1 + w * QROWS + r8 * 20 + i4 * 4]);
        a0 = gemv_mfma(wr[4 * i4 + 0], xd[0], msk16, a0);
        a1 = gemv_mfma(wr[4 * i4 + 1], xd[1], msk16, a1);
        a0 = gemv_mfma(wr[4 * i4 + 2], xd[2], msk16, a0);
        a1 = gemv_mfma(wr[4 * i4 + 3], xd[3], msk16, a1);
      }
      a0 += a1;
      if ((r >> 1) == (c >> 3)) {
#pragma unroll
        for (int g = 0; g < 4; ++g) ldsf[L_HP + w * 128 + (r >> 1) * 64 + 8 * (4 * (r & 1) + g) + c8] = a0[g];
      }
      __builtin_amdgcn_fence(__ATOMIC_RELEASE, "workgroup", "local");
      if (lane == 0) atomicAdd(&lds[L_CNT + 1], 1u);
      while (__hip_atomic_load(&lds[L_CNT + 1], __ATOMIC_RELAXED, __HIP_MEMORY_SCOPE_WORKGROUP) < (unsigned)NHLP) __builtin_amdgcn_s_sleep(2);
      __builtin_amdgcn_fence(__ATOMIC_ACQUIRE, "workgroup", "local");
      unsigned hd = 0u;
      {
        const unsigned off = FS * js - chunk0 * 8;
        float sm = 0.f;
#pragma unroll
        for (int ww = 0; ww < 2 * NHLP; ++ww) sm += ldsf[L_HP + ww * 64 + ((off + lane) & 63)];
        const float hb = rbf(rbf(sm) + bf1(b1u));
        const float u2 = 1.5957691216057308f * (hb + 0.044715f * hb * hb * hb);
        const unsigned hv = tobf(hb / (1.0f + __expf(-u2)));
        hd = (lane < (unsigned)FS) ? (hv | (hv << 16)) : 0u;
      }
      unsigned hg[4];
#pragma unroll
      for (int g = 0; g < 4; ++g) hg[g] = __builtin_amdgcn_ds_bpermute((int)((16 * w + 4 * g + r) * 4), (int)hd);
#pragma unroll
      for (int s = 0; s < 5; ++s) {
        f32x4 mm = {0.f, 0.f, 0.f, 0.f};
#pragma unroll
        for (int g = 0; g < 4; ++g) mm = gemv_mfma(wr[20 + 5 * g + s], hg[g], msk8, mm);
        if (c < 8) *reinterpret_cast<f32x4*>(&ldsf[L_PP + ((w * 10 + 5 + s) * 32 + r * 8 + c) * 4]) = mm;
      }
    }
    SB
    {
      const char* wop = wo + (size_t)(h * DH + 32 * w) * (DM * 2);
      const unsigned wlo = r * (DM * 2) + c * 16;
#pragma unroll
      for (int u = 0; u < 40; ++u) { wr[u] = ld_w(wop + (u / 5) * (4 * DM * 2) + (u % 5) * 256 + wlo); PACE }
    }
    SB
    __syncthreads();
    unsigned od[8];
    {
      float mv[NSTR], lv[NSTR], av[NSTR];
#pragma unroll
      for (int ww = 0; ww < NSTR; ++ww) {
        const float* rec = &ldsf[L_MG + ww * MG_REC];
        mv[ww] = rec[0]; lv[ww] = rec[1]; av[ww] = rec[4 + 32 * w + (lane & 31)];
      }
      float M = mv[0];
#pragma unroll
      for (int ww = 1; ww < NSTR; ++ww) M = fmaxf(M, mv[ww]);
      float L = 0.f, o = 0.f;
#pragma unroll
      for (int ww = 0; ww < NSTR; ++ww) {
        const float wt = __builtin_amdgcn_exp2f(mv[ww] - M);
        L = fmaf(wt, lv[ww], L);
        o = fmaf(wt, av[ww], o);
      }
      const unsigned ob = tobf(o / L);
      const unsigned odup = ob | (ob << 16);
#pragma unroll
      for (int g = 0; g < 8; ++g) od[g] = __builtin_amdgcn_ds_bpermute((int)((4 * g + r) * 4), (int)odup);
    }
    {
      f32x4 a[5];
#pragma unroll
      for (int s = 0; s < 5; ++s) a[s] = f32x4{0.f, 0.f, 0.f, 0.f};
#pragma unroll
      for (int g = 0; g < 8; ++g) {
#pragma unroll
        for (int s = 0; s < 5; ++s) a[s] = gemv_mfma(wr[5 * g + s], od[g], msk8, a[s]);
      }
      if (c < 8) {
#pragma unroll
        for (int s = 0; s < 5; ++s) *reinterpret_cast<f32x4*>(&ldsf[L_PP + ((w * 10 + s) * 32 + r * 8 + c) * 4]) = a[s];
      }
    }
    __syncthreads();
  }
  if (tid < 320) {
    f32x4 s = {0.f, 0.f, 0.f, 0.f};
#pragma unroll
    for (int ww = 0; ww < NHLP; ++ww) s += *reinterpret_cast<const f32x4*>(&ldsf[L_PP + (ww * 320 + tid) * 4]);
    *reinterpret_cast<f32x4*>(spart + (size_t)bid * 1280 + tid * 4) = s;
  }
}

__global__ void __launch_bounds__(256) final_kernel(const unsigned short* __restrict__ x, const unsigned short* __restrict__ b2,
                                                    const float* __restrict__ spart, unsigned short* __restrict__ out) {
  __shared__ __attribute__((aligned(16))) float red[2][16][64];
  const int b = blockIdx.x / 10, seg = blockIdx.x % 10;
  const int tid = threadIdx.x, cq = tid & 15, g = tid >> 4;
  const size_t base = (size_t)(b * NH + 2 * g) * 1280 + seg * 64 + cq * 4;
  const f32x4 s0 = *reinterpret_cast<const f32x4*>(spart + base);
  const f32x4 s1 = *reinterpret_cast<const f32x4*>(spart + base + 1280);
  const f32x4 m0 = *reinterpret_cast<const f32x4*>(spart + base + 640);
  const f32x4 m1 = *reinterpret_cast<const f32x4*>(spart + base + 1280 + 640);
  const int p = seg * 64 + (tid & 63);
  const int n = (p >> 7) * 128 + 8 * (4 * ((p >> 5) & 3) + (p & 3)) + ((p >> 2) & 7);
  float b2v = bf1(b2[n]);
  float xv = bf1(x[b * DM + n]);
  asm volatile("" : "+v"(b2v), "+v"(xv));
  *reinterpret_cast<f32x4*>(&red[0][g][cq * 4]) = s0 + s1;
  *reinterpret_cast<f32x4*>(&red[1][g][cq * 4]) = m0 + m1;
  __syncthreads();
  if (tid < 64) {
    float s = 0.f, mm = 0.f;
#pragma unroll
    for (int k = 0; k < 16; ++k) { s += red[0][k][tid]; mm += red[1][k][tid]; }
    const float sb = rbf(s);
    const float mb = rbf(rbf(mm) + b2v);
    const float gt = rbf(mb / (1.0f + __expf(-mb)));
    const float res = xv + rbf(sb * gt);
    out[b * DM + n] = tobf(res);
  }
}

}

extern "C" void kernel_launch(void* const* d_in, const int* in_sizes, int n_in, void* d_out, int out_size, void* d_ws,
                              size_t ws_size, hipStream_t stream) {
  const unsigned short* x = (const unsigned short*)d_in[0];
  const char* kc = (const char*)d_in[1];
  const char* vc = (const char*)d_in[2];
  const char* wq = (const char*)d_in[3];
  const char* wo = (const char*)d_in[4];
  const char* w1 = (const char*)d_in[6];
  const unsigned short* b1 = (const unsigned short*)d_in[7];
  const char* w2 = (const char*)d_in[8];
  const unsigned short* b2 = (const unsigned short*)d_in[9];
  float* spart = (float*)d_ws;
  fused_attn_kernel<<<NB * NH, NT, 0, stream>>>(x, kc, vc, wq, wo, w1, b1, w2, spart);
  final_kernel<<<NB * 10, 256, 0, stream>>>(x, b2, spart, (unsigned short*)d_out);
}
```

```cpp
#define WINN 4
#include <hip/hip_runtime.h>
#include <hip/hip_bf16.h>
#include <stdint.h>

namespace {

constexpr int NB = 8, DM = 640, NH = 32, SK = 4096, DH = 128, FF = 1728;
constexpr int NW = 8;
constexpr int NT = NW * 64;
constexpr int NHLP = 4, NSTR = 4;
constexpr int FS = FF / NH;
constexpr int QROWS = DM / NHLP;

typedef unsigned u32x4 __attribute__((ext_vector_type(4)));
typedef float f32x4 __attribute__((ext_vector_type(4)));
typedef __bf16 bf16x2 __attribute__((ext_vector_type(2)));
typedef __bf16 bf16x8 __attribute__((ext_vector_type(8)));

__device__ __forceinline__ float bf_lo(unsigned u) { return __builtin_bit_cast(float, u << 16); }
__device__ __forceinline__ float bf_hi(unsigned u) { return __builtin_bit_cast(float, u & 0xffff0000u); }
__device__ __forceinline__ float bf1(unsigned short u) { return __builtin_bit_cast(float, (unsigned)u << 16); }
__device__ __forceinline__ float rbf(float x) { return (float)(__bf16)x; }
__device__ __forceinline__ unsigned short tobf(float x) { return __builtin_bit_cast(unsigned short, (__bf16)x); }
__device__ __forceinline__ unsigned pack2(float lo, float hi) { return (unsigned)tobf(lo) | ((unsigned)tobf(hi) << 16); }

__device__ __forceinline__ float dot2(unsigned k, unsigned q, float acc) {
  return __builtin_amdgcn_fdot2_f32_bf16(__builtin_bit_cast(bf16x2, k), __builtin_bit_cast(bf16x2, q), acc, false);
}
template <int CTRL> __device__ __forceinline__ float dpp(float x) {
  return __builtin_bit_cast(float, __builtin_amdgcn_mov_dpp(__builtin_bit_cast(int, x), CTRL, 0xf, 0xf, true));
}
__device__ __forceinline__ float row16_sum(float x) {
  x += dpp<0xB1>(x);
  x += dpp<0x4E>(x);
  x += dpp<0x141>(x);
  x += dpp<0x140>(x);
  return x;
}
__device__ __forceinline__ float xrow_sum(float x) {
  auto s = __builtin_amdgcn_permlane16_swap(__float_as_uint(x), __float_as_uint(x), false, false);
  x = __uint_as_float(s[0]) + __uint_as_float(s[1]);
  auto t = __builtin_amdgcn_permlane32_swap(__float_as_uint(x), __float_as_uint(x), false, false);
  return __uint_as_float(t[0]) + __uint_as_float(t[1]);
}
__device__ __forceinline__ float xrow_max(float x) {
  auto s = __builtin_amdgcn_permlane16_swap(__float_as_uint(x), __float_as_uint(x), false, false);
  x = fmaxf(__uint_as_float(s[0]), __uint_as_float(s[1]));
  auto t = __builtin_amdgcn_permlane32_swap(__float_as_uint(x), __float_as_uint(x), false, false);
  return fmaxf(__uint_as_float(t[0]), __uint_as_float(t[1]));
}
__device__ __forceinline__ float wave_sum(float x) { return xrow_sum(row16_sum(x)); }

__device__ __forceinline__ u32x4 ld_nt(const char* p) { return __builtin_nontemporal_load(reinterpret_cast<const u32x4*>(p)); }
__device__ __forceinline__ u32x4 ld_w(const char* p) { return __builtin_nontemporal_load(reinterpret_cast<const u32x4*>(p)); }

__device__ __forceinline__ f32x4 gemv_mfma(u32x4 a, unsigned xdup, u32x4 msk, f32x4 c) {
  const u32x4 bsel = {xdup & msk[0], xdup & msk[1], xdup & msk[2], xdup & msk[3]};
  return __builtin_amdgcn_mfma_f32_16x16x32_bf16(__builtin_bit_cast(bf16x8, a), __builtin_bit_cast(bf16x8, bsel), c, 0, 0, 0);
}

constexpr int L_XQ = 0;
constexpr int L_X1 = L_XQ + DM;
constexpr int L_QP = L_X1 + DM;
constexpr int L_HP = L_QP + NHLP * DH;
constexpr int L_MG = L_HP + NHLP * 128;
constexpr int MG_REC = DH + 4;
constexpr int L_PP = L_MG + NSTR * MG_REC;
constexpr int L_SC = L_PP + NHLP * 10 * 128;
constexpr int L_CNT = L_SC + NSTR * 1024;
constexpr int L_END = L_CNT + 4;

__global__ void __launch_bounds__(NT, 2) fused_attn_kernel(
    const unsigned short* __restrict__ x, const char* __restrict__ kc, const char* __restrict__ vc,
    const char* __restrict__ wq, const char* __restrict__ wo, const char* __restrict__ w1,
    const unsigned short* __restrict__ b1, const char* __restrict__ w2, float* __restrict__ spart) {
  __shared__ __attribute__((aligned(16))) unsigned lds[L_END];
  float* ldsf = reinterpret_cast<float*>(lds);
  const int tid = threadIdx.x;
  const unsigned lane = tid & 63;
  const int w = __builtin_amdgcn_readfirstlane(tid >> 6);
  const unsigned r = lane >> 4, c = lane & 15;
  const int bid = blockIdx.x;
  const int h = bid & 31, b = bid >> 5;
#define SB __builtin_amdgcn_sched_barrier(0);
#define PACE SB __builtin_amdgcn_s_sleep(20); SB

  if (w >= NHLP) {
    const int j = w - NHLP;
    const char* kb = kc + (size_t)bid * (SK * DH * 2) + j * 4096;
    const char* vb = vc + (size_t)bid * (SK * DH * 2) + j * 4096;
    const unsigned lo16 = lane * 16;
    u32x4 R[8][4];
#define LOAD_P(d, base) _Pragma("unroll") for (int t = 0; t < 4; ++t) R[d][t] = ld_nt((base) + (d) * 16384 + t * 1024 + lo16);
#define STR2(x) #x
#define STR(x) STR2(x)
#define REFILL1(d, t, base, lim) {                                      \
      asm volatile("s_cmp_eq_u32 %0, 0\n\ts_cbranch_scc1 1f\n\ts_waitcnt vmcnt(" STR(WINN) ")\n1:" : : "s"(lim) : "scc", "memory"); \
      R[d][t] = ld_nt((base) + (d) * 16384 + (t) * 1024 + lo16); SB }
#pragma unroll
    for (int d = 0; d < 8; ++d) { LOAD_P(d, kb) SB }
    __syncthreads();
    u32x4 q;
    {
      const float* qp = &ldsf[L_QP + r * DH + c * 8];
      f32x4 s0 = *reinterpret_cast<const f32x4*>(qp);
      f32x4 s1 = *reinterpret_cast<const f32x4*>(qp + 4);
#pragma unroll
      for (int k = 0; k < 4; ++k) { s0[k] = xrow_sum(s0[k]); s1[k] = xrow_sum(s1[k]); }
      q[0] = pack2(s0[0], s0[1]); q[1] = pack2(s0[2], s0[3]); q[2] = pack2(s1[0], s1[1]); q[3] = pack2(s1[2], s1[3]);
    }
    asm volatile("" : "+v"(q));

    float* scw = &ldsf[L_SC + j * 1024];
    const bool c1 = (c & 1) != 0, c2 = (c & 2) != 0;
    float mx = -INFINITY;
    {
      float* sp = scw + r * 4 + (c & 3);
      const char* nb = kb + 8 * 16384;
      for (int it = 0; it < 8; ++it) {
        if (it == 7) nb = vb;
#pragma unroll
        for (int d = 0; d < 8; ++d) {
          float s_[4];
#pragma unroll
          for (int t = 0; t < 4; ++t) {
            float dd = 0.f;
#pragma unroll
            for (int k = 0; k < 4; ++k) dd = dot2(R[d][t][k], q[k], dd);
            asm volatile("" : "+v"(dd));
            s_[t] = dd;
            REFILL1(d, t, nb, 1)
          }
          SB
          const float x0 = (c1 ? s_[1] : s_[0]) + dpp<0xB1>(c1 ? s_[0] : s_[1]);
          const float x1 = (c1 ? s_[3] : s_[2]) + dpp<0xB1>(c1 ? s_[2] : s_[3]);
          float y = (c2 ? x1 : x0) + dpp<0x4E>(c2 ? x0 : x1);
          y += dpp<0x128>(y);
          y += dpp<0x124>(y);
          mx = fmaxf(mx, y);
          if (c < 4) sp[d * 16] = y;
          SB
        }
        sp += 128;
        nb += 8 * 16384;
      }
    }
    constexpr float C2 = 0.08838834764831845f * 1.4426950408889634f;
    float m, l;
    {
      mx = fmaxf(mx, dpp<0xB1>(mx));
      mx = fmaxf(mx, dpp<0x4E>(mx));
      m = xrow_max(mx) * C2;
      __builtin_amdgcn_fence(__ATOMIC_RELEASE, "wavefront");
      __builtin_amdgcn_wave_barrier();
      __builtin_amdgcn_fence(__ATOMIC_ACQUIRE, "wavefront");
      float ls = 0.f;
#pragma unroll
      for (int u = 0; u < 4; ++u) {
        f32x4 v = *reinterpret_cast<const f32x4*>(scw + u * 256 + lane * 4);
#pragma unroll
        for (int k = 0; k < 4; ++k) v[k] = __builtin_amdgcn_exp2f(fmaf(v[k], C2, -m));
        ls += (v[0] + v[1]) + (v[2] + v[3]);
        *reinterpret_cast<f32x4*>(scw + u * 256 + lane * 4) = v;
      }
      l = wave_sum(ls);
      __builtin_amdgcn_fence(__ATOMIC_RELEASE, "wavefront");
      __builtin_amdgcn_wave_barrier();
      __builtin_amdgcn_fence(__ATOMIC_ACQUIRE, "wavefront");
    }
    float acc[8];
#pragma unroll
    for (int k = 0; k < 8; ++k) acc[k] = 0.f;
#define PV_PIECE(d, pp, RF)                                           \
  {                                                                   \
    const f32x4 p_ = *reinterpret_cast<const f32x4*>((pp) + (d) * 16); \
    _Pragma("unroll") for (int t = 0; t < 4; ++t) {                   \
      _Pragma("unroll") for (int k = 0; k < 4; ++k) {                 \
        acc[2 * k] = fmaf(p_[t], bf_lo(R[d][t][k]), acc[2 * k]);      \
        acc[2 * k + 1] = fmaf(p_[t], bf_hi(R[d][t][k]), acc[2 * k + 1]); \
      }                                                               \
      asm volatile("" : "+v"(acc[0]), "+v"(acc[1]), "+v"(acc[2]), "+v"(acc[3]), "+v"(acc[4]), "+v"(acc[5]), "+v"(acc[6]), "+v"(acc[7])); \
      if (RF) REFILL1(d, t, nb, 6 - it)                               \
    }                                                                 \
  }
    {
      const float* pp = scw + r * 4;
      const char* nb = vb + 8 * 16384;
      for (int it = 0; it < 7; ++it) {
#pragma unroll
        for (int d = 0; d < 8; ++d) { PV_PIECE(d, pp, 1) SB }
        pp += 128;
        nb += 8 * 16384;
      }
      const int it = 0;
#pragma unroll
      for (int d = 0; d < 8; ++d) { PV_PIECE(d, pp, 0) SB }
    }
    {
#pragma unroll
      for (int k = 0; k < 8; ++k) acc[k] = xrow_sum(acc[k]);
      float* rec = &ldsf[L_MG + j * MG_REC];
      if (r == 0) {
        *reinterpret_cast<f32x4*>(rec + 4 + c * 8) = f32x4{acc[0], acc[1], acc[2], acc[3]};
        *reinterpret_cast<f32x4*>(rec + 4 + c * 8 + 4) = f32x4{acc[4], acc[5], acc[6], acc[7]};
        if (c == 0) { rec[0] = m; rec[1] = l; }
      }
    }
    __syncthreads();
    __syncthreads();
  } else {
    __builtin_amdgcn_s_setprio(3);
    const int js = (h & 7) * 4 + (h >> 3);
    const unsigned r8 = lane >> 3, c8 = lane & 7;
    const int chunk0 = (FS * js) >> 3;
    const unsigned short* xb = x + b * DM;
    const u32x4 xa = *reinterpret_cast<const u32x4*>(xb + lane * 8);
    const unsigned xt = *reinterpret_cast<const unsigned*>(xb + 512 + lane * 2);
    const unsigned short xo0 = xb[QROWS * w + lane];
    const unsigned short xo1 = xb[QROWS * w + 64 + lane];
    const unsigned short xo2 = xb[QROWS * w + 128 + (lane & 31)];
    const unsigned short b1u = b1[FS * js + min(lane, (unsigned)(FS - 1))];
    SB
    u32x4 wr[40];
    {
      const char* p = wq + (size_t)(QROWS * w) * (NH * DH * 2) + h * (DH * 2);
      const unsigned lo = r * (NH * DH * 2) + c * 16;
#pragma unroll
      for (int i = 0; i < 40; ++i) {
        wr[i] = ld_w(p + (size_t)(4 * i) * (NH * DH * 2) + lo);
        if ((i & 3) == 0) { SB }
      }
    }
    SB
    if (tid == 0) lds[L_CNT + 1] = 0;
    u32x4 msk8, msk16;
#pragma unroll
    for (int k = 0; k < 4; ++k) {
      const unsigned m16 = (c8 == 2u * k) ? 0x0000ffffu : ((c8 == 2u * k + 1) ? 0xffff0000u : 0u);
      msk16[k] = m16;
      msk8[k] = (c < 8) ? m16 : 0u;
    }
    {
      float xv[10];
#pragma unroll
      for (int k = 0; k < 4; ++k) { xv[2 * k] = bf_lo(xa[k]); xv[2 * k + 1] = bf_hi(xa[k]); }
      xv[8] = bf_lo(xt); xv[9] = bf_hi(xt);
      float sm = 0.f;
#pragma unroll
      for (int k = 0; k < 10; ++k) sm += xv[k];
      const float mean = wave_sum(sm) * (1.0f / DM);
      float ss = 0.f;
#pragma unroll
      for (int k = 0; k < 10; ++k) { const float d = xv[k] - mean; ss = fmaf(d, d, ss); }
      const float var = wave_sum(ss) * (1.0f / DM);
      const float mb = rbf(mean);
      const float sd = rbf(sqrtf(rbf(rbf(var) + 1e-5f)));
      const unsigned n0 = tobf(rbf(bf1(xo0) - mb) / sd);
      const unsigned n1 = tobf(rbf(bf1(xo1) - mb) / sd);
      const unsigned n2 = tobf(rbf(bf1(xo2) - mb) / sd);
      const unsigned d0 = n0 | (n0 << 16), d1 = n1 | (n1 << 16), d2 = n2 | (n2 << 16);
      unsigned* xq = &lds[L_XQ + w * QROWS + (lane & 3) * 40 + (lane >> 2)];
      unsigned* x1 = &lds[L_X1 + w * QROWS + (lane & 7) * 20 + (lane >> 3)];
      xq[0] = d0; xq[16] = d1;
      x1[0] = d0; x1[8] = d1;
      if (lane < 32) { xq[32] = d2; x1[16] = d2; }
    }
    __builtin_amdgcn_fence(__ATOMIC_RELEASE, "wavefront");
    __builtin_amdgcn_wave_barrier();
    __builtin_amdgcn_fence(__ATOMIC_ACQUIRE, "wavefront");
    {
      f32x4 a0 = {0.f, 0.f, 0.f, 0.f}, a1 = {0.f, 0.f, 0.f, 0.f};
#pragma unroll
      for (int i4 = 0; i4 < 10; ++i4) {
        const u32x4 xd = *reinterpret_cast<const u32x4*>(&lds[L_XQ + w * QROWS + r * 40 + i4 * 4]);
        a0 = gemv_mfma(wr[4 * i4 + 0], xd[0], msk8, a0);
        a1 = gemv_mfma(wr[4 * i4 + 1], xd[1], msk8, a1);
        a0 = gemv_mfma(wr[4 * i4 + 2], xd[2], msk8, a0);
        a1 = gemv_mfma(wr[4 * i4 + 3], xd[3], msk8, a1);
      }
      a0 += a1;
      if (c < 8) {
#pragma unroll
        for (int g = 0; g < 4; ++g) ldsf[L_QP + w * DH + 8 * (4 * r + g) + c] = a0[g];
      }
    }
    __builtin_amdgcn_s_setprio(0);
    __syncthreads();
    {
      const unsigned ch = min((unsigned)chunk0 + c8, (unsigned)(FF / 8 - 1));
      const char* p = w1 + (size_t)(QROWS * w) * (FF * 2);
      const unsigned lo = r8 * (FF * 2) + ch * 16;
#pragma unroll
      for (int i = 0; i < 20; ++i) { wr[i] = ld_w(p + (size_t)(8 * i) * (FF * 2) + lo); PACE }
      SB
      const char* p2 = w2 + (size_t)(FS * js) * (DM * 2);
#pragma unroll
      for (int g = 0; g < 4; ++g) {
        const unsigned lo2 = min(16u * w + 4 * g + r, (unsigned)(FS - 1)) * (DM * 2) + c * 16;
#pragma unroll
        for (int s = 0; s < 5; ++s) { wr[20 + 5 * g + s] = ld_w(p2 + s * 256 + lo2); PACE }
      }
      SB
      f32x4 a0 = {0.f, 0.f, 0.f, 0.f}, a1 = {0.f, 0.f, 0.f, 0.f};
#pragma unroll
      for (int i4 = 0; i4 < 5; ++i4) {
        const u32x4 xd = *reinterpret_cast<const u32x4*>(&lds[L_X1 + w * QROWS + r8 * 20 + i4 * 4]);
        a0 = gemv_mfma(wr[4 * i4 + 0], xd[0], msk16, a0);
        a1 = gemv_mfma(wr[4 * i4 + 1], xd[1], msk16, a1);
        a0 = gemv_mfma(wr[4 * i4 + 2], xd[2], msk16, a0);
        a1 = gemv_mfma(wr[4 * i4 + 3], xd[3], msk16, a1);
      }
      a0 += a1;
      if ((r >> 1) == (c >> 3)) {
#pragma unroll
        for (int g = 0; g < 4; ++g) ldsf[L_HP + w * 128 + (r >> 1) * 64 + 8 * (4 * (r & 1) + g) + c8] = a0[g];
      }
      __builtin_amdgcn_fence(__ATOMIC_RELEASE, "workgroup", "local");
      if (lane == 0) atomicAdd(&lds[L_CNT + 1], 1u);
      while (__hip_atomic_load(&lds[L_CNT + 1], __ATOMIC_RELAXED, __HIP_MEMORY_SCOPE_WORKGROUP) < (unsigned)NHLP) __builtin_amdgcn_s_sleep(2);
      __builtin_amdgcn_fence(__ATOMIC_ACQUIRE, "workgroup", "local");
      unsigned hd = 0u;
      {
        const unsigned off = FS * js - chunk0 * 8;
        float sm = 0.f;
#pragma unroll
        for (int ww = 0; ww < 2 * NHLP; ++ww) sm += ldsf[L_HP + ww * 64 + ((off + lane) & 63)];
        const float hb = rbf(rbf(sm) + bf1(b1u));
        const float u2 = 1.5957691216057308f * (hb + 0.044715f * hb * hb * hb);
        const unsigned hv = tobf(hb / (1.0f + __expf(-u2)));
        hd = (lane < (unsigned)FS) ? (hv | (hv << 16)) : 0u;
      }
      unsigned hg[4];
#pragma unroll
      for (int g = 0; g < 4; ++g) hg[g] = __builtin_amdgcn_ds_bpermute((int)((16 * w + 4 * g + r) * 4), (int)hd);
#pragma unroll
      for (int s = 0; s < 5; ++s) {
        f32x4 mm = {0.f, 0.f, 0.f, 0.f};
#pragma unroll
        for (int g = 0; g < 4; ++g) mm = gemv_mfma(wr[20 + 5 * g + s], hg[g], msk8, mm);
        if (c < 8) *reinterpret_cast<f32x4*>(&ldsf[L_PP + ((w * 10 + 5 + s) * 32 + r * 8 + c) * 4]) = mm;
      }
    }
    SB
    {
      const char* wop = wo + (size_t)(h * DH + 32 * w) * (DM * 2);
      const unsigned wlo = r * (DM * 2) + c * 16;
#pragma unroll
      for (int u = 0; u < 40; ++u) { wr[u] = ld_w(wop + (u / 5) * (4 * DM * 2) + (u % 5) * 256 + wlo); PACE }
    }
    SB
    __syncthreads();
    unsigned od[8];
    {
      float mv[NSTR], lv[NSTR], av[NSTR];
#pragma unroll
      for (int ww = 0; ww < NSTR; ++ww) {
        const float* rec = &ldsf[L_MG + ww * MG_REC];
        mv[ww] = rec[0]; lv[ww] = rec[1]; av[ww] = rec[4 + 32 * w + (lane & 31)];
      }
      float M = mv[0];
#pragma unroll
      for (int ww = 1; ww < NSTR; ++ww) M = fmaxf(M, mv[ww]);
      float L = 0.f, o = 0.f;
#pragma unroll
      for (int ww = 0; ww < NSTR; ++ww) {
        const float wt = __builtin_amdgcn_exp2f(mv[ww] - M);
        L = fmaf(wt, lv[ww], L);
        o = fmaf(wt, av[ww], o);
      }
      const unsigned ob = tobf(o / L);
      const unsigned odup = ob | (ob << 16);
#pragma unroll
      for (int g = 0; g < 8; ++g) od[g] = __builtin_amdgcn_ds_bpermute((int)((4 * g + r) * 4), (int)odup);
    }
    {
      f32x4 a[5];
#pragma unroll
      for (int s = 0; s < 5; ++s) a[s] = f32x4{0.f, 0.f, 0.f, 0.f};
#pragma unroll
      for (int g = 0; g < 8; ++g) {
#pragma unroll
        for (int s = 0; s < 5; ++s) a[s] = gemv_mfma(wr[5 * g + s], od[g], msk8, a[s]);
      }
      if (c < 8) {
#pragma unroll
        for (int s = 0; s < 5; ++s) *reinterpret_cast<f32x4*>(&ldsf[L_PP + ((w * 10 + s) * 32 + r * 8 + c) * 4]) = a[s];
      }
    }
    __syncthreads();
  }
  if (tid < 320) {
    f32x4 s = {0.f, 0.f, 0.f, 0.f};
#pragma unroll
    for (int ww = 0; ww < NHLP; ++ww) s += *reinterpret_cast<const f32x4*>(&ldsf[L_PP + (ww * 320 + tid) * 4]);
    *reinterpret_cast<f32x4*>(spart + (size_t)bid * 1280 + tid * 4) = s;
  }
}

__global__ void __launch_bounds__(256) final_kernel(const unsigned short* __restrict__ x, const unsigned short* __restrict__ b2,
                                                    const float* __restrict__ spart, unsigned short* __restrict__ out) {
  __shared__ __attribute__((aligned(16))) float red[2][16][64];
  const int b = blockIdx.x / 10, seg = blockIdx.x % 10;
  const int tid = threadIdx.x, cq = tid & 15, g = tid >> 4;
  const size_t base = (size_t)(b * NH + 2 * g) * 1280 + seg * 64 + cq * 4;
  const f32x4 s0 = *reinterpret_cast<const f32x4*>(spart + base);
  const f32x4 s1 = *reinterpret_cast<const f32x4*>(spart + base + 1280);
  const f32x4 m0 = *reinterpret_cast<const f32x4*>(spart + base + 640);
  const f32x4 m1 = *reinterpret_cast<const f32x4*>(spart + base + 1280 + 640);
  const int p = seg * 64 + (tid & 63);
  const int n = (p >> 7) * 128 + 8 * (4 * ((p >> 5) & 3) + (p & 3)) + ((p >> 2) & 7);
  float b2v = bf1(b2[n]);
  float xv = bf1(x[b * DM + n]);
  asm volatile("" : "+v"(b2v), "+v"(xv));
  *reinterpret_cast<f32x4*>(&red[0][g][cq * 4]) = s0 + s1;
  *reinterpret_cast<f32x4*>(&red[1][g][cq * 4]) = m0 + m1;
  __syncthreads();
  if (tid < 64) {
    float s = 0.f, mm = 0.f;
#pragma unroll
    for (int k = 0; k < 16; ++k) { s += red[0][k][tid]; mm += red[1][k][tid]; }
    const float sb = rbf(s);
    const float mb = rbf(rbf(mm) + b2v);
    const float gt = rbf(mb / (1.0f + __expf(-mb)));
    const float res = xv + rbf(sb * gt);
    out[b * DM + n] = tobf(res);
  }
}

}

extern "C" void kernel_launch(void* const* d_in, const int* in_sizes, int n_in, void* d_out, int out_size, void* d_ws,
                              size_t ws_size, hipStream_t stream) {
  const unsigned short* x = (const unsigned short*)d_in[0];
  const char* kc = (const char*)d_in[1];
  const char* vc = (const char*)d_in[2];
  const char* wq = (const char*)d_in[3];
  const char* wo = (const char*)d_in[4];
  const char* w1 = (const char*)d_in[6];
  const unsigned short* b1 = (const unsigned short*)d_in[7];
  const char* w2 = (const char*)d_in[8];
  const unsigned short* b2 = (const unsigned short*)d_in[9];
  float* spart = (float*)d_ws;
  fused_attn_kernel<<<NB * NH, NT, 0, stream>>>(x, kc, vc, wq, wo, w1, b1, w2, spart);
  final_kernel<<<NB * 10, 256, 0, stream>>>(x, b2, spart, (unsigned short*)d_out);
}
```

```cpp
#define DP 58
#define WINN 4
#define LOOK 2
#include <hip/hip_runtime.h>
#include <hip/hip_bf16.h>
#include <stdint.h>

namespace {

constexpr int NB = 8, DM = 640, NH = 32, SK = 4096, DH = 128, FF = 1728;
constexpr int NW = 8;
constexpr int NT = NW * 64;
constexpr int NHLP = 4, NSTR = 4;
constexpr int FS = FF / NH;
constexpr int QROWS = DM / NHLP;

typedef unsigned u32x4 __attribute__((ext_vector_type(4)));
typedef float f32x4 __attribute__((ext_vector_type(4)));
typedef __bf16 bf16x2 __attribute__((ext_vector_type(2)));
typedef __bf16 bf16x8 __attribute__((ext_vector_type(8)));

__device__ __forceinline__ float bf_lo(unsigned u) { return __builtin_bit_cast(float, u << 16); }
__device__ __forceinline__ float bf_hi(unsigned u) { return __builtin_bit_cast(float, u & 0xffff0000u); }
__device__ __forceinline__ float bf1(unsigned short u) { return __builtin_bit_cast(float, (unsigned)u << 16); }
__device__ __forceinline__ float rbf(float x) { return (float)(__bf16)x; }
__device__ __forceinline__ unsigned short tobf(float x) { return __builtin_bit_cast(unsigned short, (__bf16)x); }
__device__ __forceinline__ unsigned pack2(float lo, float hi) { return (unsigned)tobf(lo) | ((unsigned)tobf(hi) << 16); }

__device__ __forceinline__ float dot2(unsigned k, unsigned q, float acc) {
  return __builtin_amdgcn_fdot2_f32_bf16(__builtin_bit_cast(bf16x2, k), __builtin_bit_cast(bf16x2, q), acc, false);
}
template <int CTRL> __device__ __forceinline__ float dpp(float x) {
  return __builtin_bit_cast(float, __builtin_amdgcn_mov_dpp(__builtin_bit_cast(int, x), CTRL, 0xf, 0xf, true));
}
__device__ __forceinline__ float row16_sum(float x) {
  x += dpp<0xB1>(x);
  x += dpp<0x4E>(x);
  x += dpp<0x141>(x);
  x += dpp<0x140>(x);
  return x;
}
__device__ __forceinline__ float xrow_sum(float x) {
  auto s = __builtin_amdgcn_permlane16_swap(__float_as_uint(x), __float_as_uint(x), false, false);
  x = __uint_as_float(s[0]) + __uint_as_float(s[1]);
  auto t = __builtin_amdgcn_permlane32_swap(__float_as_uint(x), __float_as_uint(x), false, false);
  return __uint_as_float(t[0]) + __uint_as_float(t[1]);
}
__device__ __forceinline__ float xrow_max(float x) {
  auto s = __builtin_amdgcn_permlane16_swap(__float_as_uint(x), __float_as_uint(x), false, false);
  x = fmaxf(__uint_as_float(s[0]), __uint_as_float(s[1]));
  auto t = __builtin_amdgcn_permlane32_swap(__float_as_uint(x), __float_as_uint(x), false, false);
  return fmaxf(__uint_as_float(t[0]), __uint_as_float(t[1]));
}
__device__ __forceinline__ float wave_sum(float x) { return xrow_sum(row16_sum(x)); }

__device__ __forceinline__ u32x4 ld_nt(const char* p) { return __builtin_nontemporal_load(reinterpret_cast<const u32x4*>(p)); }
__device__ __forceinline__ u32x4 ld_w(const char* p) { return __builtin_nontemporal_load(reinterpret_cast<const u32x4*>(p)); }

__device__ __forceinline__ f32x4 gemv_mfma(u32x4 a, unsigned xdup, u32x4 msk, f32x4 c) {
  const u32x4 bsel = {xdup & msk[0], xdup & msk[1], xdup & msk[2], xdup & msk[3]};
  return __builtin_amdgcn_mfma_f32_16x16x32_bf16(__builtin_bit_cast(bf16x8, a), __builtin_bit_cast(bf16x8, bsel), c, 0, 0, 0);
}

constexpr int L_XQ = 0;
constexpr int L_X1 = L_XQ + DM;
constexpr int L_QP = L_X1 + DM;
constexpr int L_HP = L_QP + NHLP * DH;
constexpr int L_MG = L_HP + NHLP * 128;
constexpr int MG_REC = DH + 4;
constexpr int L_PP = L_MG + NSTR * MG_REC;
constexpr int L_SC = L_PP + NHLP * 10 * 128;
constexpr int L_CNT = L_SC + NSTR * 1024;
constexpr int L_END = L_CNT + 4;

__global__ void __launch_bounds__(NT, 2) fused_attn_kernel(
    const unsigned short* __restrict__ x, const char* __restrict__ kc, const char* __restrict__ vc,
    const char* __restrict__ wq, const char* __restrict__ wo, const char* __restrict__ w1,
    const unsigned short* __restrict__ b1, const char* __restrict__ w2, float* __restrict__ spart) {
  __shared__ __attribute__((aligned(16))) unsigned lds[L_END];
  float* ldsf = reinterpret_cast<float*>(lds);
  const int tid = threadIdx.x;
  const unsigned lane = tid & 63;
  const int w = __builtin_amdgcn_readfirstlane(tid >> 6);
  const unsigned r = lane >> 4, c = lane & 15;
  const int bid = blockIdx.x;
  const int h = bid & 31, b = bid >> 5;
#define SB __builtin_amdgcn_sched_barrier(0);
#define PACE SB __builtin_amdgcn_s_sleep(20); SB

  if (w >= NHLP) {
    const int j = w - NHLP;
    const char* kb = kc + (size_t)bid * (SK * DH * 2) + j * 4096;
    const char* vb = vc + (size_t)bid * (SK * DH * 2) + j * 4096;
    const unsigned lo16 = lane * 16;
    u32x4 R[8][4];
    unsigned tsch = (unsigned)__builtin_amdgcn_s_memrealtime() + (8 - LOOK) * DP;
    tsch = __builtin_amdgcn_readfirstlane(tsch);
#define RATE_WAIT { while ((int)((unsigned)__builtin_amdgcn_s_memrealtime() - tsch) < 0) __builtin_amdgcn_s_sleep(1); tsch += DP; SB }
#define LOAD_P(d, base) _Pragma("unroll") for (int t = 0; t < 4; ++t) R[d][t] = ld_nt((base) + (d) * 16384 + t * 1024 + lo16);
#define STR2(x) #x
#define STR(x) STR2(x)
#define REFILL1(d, t, base, lim) {                                      \
      asm volatile("s_cmp_eq_u32 %0, 0\n\ts_cbranch_scc1 1f\n\ts_waitcnt vmcnt(" STR(WINN) ")\n1:" : : "s"(lim) : "scc", "memory"); \
      R[d][t] = ld_nt((base) + (d) * 16384 + (t) * 1024 + lo16); SB }
#pragma unroll
    for (int d = 0; d < 8; ++d) { LOAD_P(d, kb) SB }
    __syncthreads();
    u32x4 q;
    {
      const float* qp = &ldsf[L_QP + r * DH + c * 8];
      f32x4 s0 = *reinterpret_cast<const f32x4*>(qp);
      f32x4 s1 = *reinterpret_cast<const f32x4*>(qp + 4);
#pragma unroll
      for (int k = 0; k < 4; ++k) { s0[k] = xrow_sum(s0[k]); s1[k] = xrow_sum(s1[k]); }
      q[0] = pack2(s0[0], s0[1]); q[1] = pack2(s0[2], s0[3]); q[2] = pack2(s1[0], s1[1]); q[3] = pack2(s1[2], s1[3]);
    }
    asm volatile("" : "+v"(q));

    float* scw = &ldsf[L_SC + j * 1024];
    const bool c1 = (c & 1) != 0, c2 = (c & 2) != 0;
    float mx = -INFINITY;
    {
      float* sp = scw + r * 4 + (c & 3);
      const char* nb = kb + 8 * 16384;
      for (int it = 0; it < 8; ++it) {
        if (it == 7) nb = vb;
#pragma unroll
        for (int d = 0; d < 8; ++d) {
          float s_[4];
          RATE_WAIT
#pragma unroll
          for (int t = 0; t < 4; ++t) {
            float dd = 0.f;
#pragma unroll
            for (int k = 0; k < 4; ++k) dd = dot2(R[d][t][k], q[k], dd);
            asm volatile("" : "+v"(dd));
            s_[t] = dd;
            REFILL1(d, t, nb, 1)
          }
          SB
          const float x0 = (c1 ? s_[1] : s_[0]) + dpp<0xB1>(c1 ? s_[0] : s_[1]);
          const float x1 = (c1 ? s_[3] : s_[2]) + dpp<0xB1>(c1 ? s_[2] : s_[3]);
          float y = (c2 ? x1 : x0) + dpp<0x4E>(c2 ? x0 : x1);
          y += dpp<0x128>(y);
          y += dpp<0x124>(y);
          mx = fmaxf(mx, y);
          if (c < 4) sp[d * 16] = y;
          SB
        }
        sp += 128;
        nb += 8 * 16384;
      }
    }
    constexpr float C2 = 0.08838834764831845f * 1.4426950408889634f;
    float m, l;
    {
      mx = fmaxf(mx, dpp<0xB1>(mx));
      mx = fmaxf(mx, dpp<0x4E>(mx));
      m = xrow_max(mx) * C2;
      __builtin_amdgcn_fence(__ATOMIC_RELEASE, "wavefront");
      __builtin_amdgcn_wave_barrier();
      __builtin_amdgcn_fence(__ATOMIC_ACQUIRE, "wavefront");
      float ls = 0.f;
#pragma unroll
      for (int u = 0; u < 4; ++u) {
        f32x4 v = *reinterpret_cast<const f32x4*>(scw + u * 256 + lane * 4);
#pragma unroll
        for (int k = 0; k < 4; ++k) v[k] = __builtin_amdgcn_exp2f(fmaf(v[k], C2, -m));
        ls += (v[0] + v[1]) + (v[2] + v[3]);
        *reinterpret_cast<f32x4*>(scw + u * 256 + lane * 4) = v;
      }
      l = wave_sum(ls);
      __builtin_amdgcn_fence(__ATOMIC_RELEASE, "wavefront");
      __builtin_amdgcn_wave_barrier();
      __builtin_amdgcn_fence(__ATOMIC_ACQUIRE, "wavefront");
    }
    float acc[8];
#pragma unroll
    for (int k = 0; k < 8; ++k) acc[k] = 0.f;
#define PV_PIECE(d, pp, RF)                                           \
  {                                                                   \
    const f32x4 p_ = *reinterpret_cast<const f32x4*>((pp) + (d) * 16); \
    _Pragma("unroll") for (int t = 0; t < 4; ++t) {                   \
      _Pragma("unroll") for (int k = 0; k < 4; ++k) {                 \
        acc[2 * k] = fmaf(p_[t], bf_lo(R[d][t][k]), acc[2 * k]);      \
        acc[2 * k + 1] = fmaf(p_[t], bf_hi(R[d][t][k]), acc[2 * k + 1]); \
      }                                                               \
      asm volatile("" : "+v"(acc[0]), "+v"(acc[1]), "+v"(acc[2]), "+v"(acc[3]), "+v"(acc[4]), "+v"(acc[5]), "+v"(acc[6]), "+v"(acc[7])); \
      if (RF) REFILL1(d, t, nb, 6 - it)                               \
    }                                                                 \
  }
    {
      const float* pp = scw + r * 4;
      const char* nb = vb + 8 * 16384;
      for (int it = 0; it < 7; ++it) {
#pragma unroll
        for (int d = 0; d < 8; ++d) { RATE_WAIT PV_PIECE(d, pp, 1) SB }
        pp += 128;
        nb += 8 * 16384;
      }
      const int it = 0;
#pragma unroll
      for (int d = 0; d < 8; ++d) { PV_PIECE(d, pp, 0) SB }
    }
    {
#pragma unroll
      for (int k = 0; k < 8; ++k) acc[k] = xrow_sum(acc[k]);
      float* rec = &ldsf[L_MG + j * MG_REC];
      if (r == 0) {
        *reinterpret_cast<f32x4*>(rec + 4 + c * 8) = f32x4{acc[0], acc[1], acc[2], acc[3]};
        *reinterpret_cast<f32x4*>(rec + 4 + c * 8 + 4) = f32x4{acc[4], acc[5], acc[6], acc[7]};
        if (c == 0) { rec[0] = m; rec[1] = l; }
      }
    }
    __syncthreads();
    __syncthreads();
  } else {
    __builtin_amdgcn_s_setprio(3);
    const int js = (h & 7) * 4 + (h >> 3);
    const unsigned r8 = lane >> 3, c8 = lane & 7;
    const int chunk0 = (FS * js) >> 3;
    const unsigned short* xb = x + b * DM;
    const u32x4 xa = *reinterpret_cast<const u32x4*>(xb + lane * 8);
    const unsigned xt = *reinterpret_cast<const unsigned*>(xb + 512 + lane * 2);
    const unsigned short xo0 = xb[QROWS * w + lane];
    const unsigned short xo1 = xb[QROWS * w + 64 + lane];
    const unsigned short xo2 = xb[QROWS * w + 128 + (lane & 31)];
    const unsigned short b1u = b1[FS * js + min(lane, (unsigned)(FS - 1))];
    SB
    u32x4 wr[40];
    {
      const char* p = wq + (size_t)(QROWS * w) * (NH * DH * 2) + h * (DH * 2);
      const unsigned lo = r * (NH * DH * 2) + c * 16;
#pragma unroll
      for (int i = 0; i < 40; ++i) {
        wr[i] = ld_w(p + (size_t)(4 * i) * (NH * DH * 2) + lo);
        if ((i & 3) == 0) { SB }
      }
    }
    SB
    if (tid == 0) lds[L_CNT + 1] = 0;
    u32x4 msk8, msk16;
#pragma unroll
    for (int k = 0; k < 4; ++k) {
      const unsigned m16 = (c8 == 2u * k) ? 0x0000ffffu : ((c8 == 2u * k + 1) ? 0xffff0000u : 0u);
      msk16[k] = m16;
      msk8[k] = (c < 8) ? m16 : 0u;
    }
    {
      float xv[10];
#pragma unroll
      for (int k = 0; k < 4; ++k) { xv[2 * k] = bf_lo(xa[k]); xv[2 * k + 1] = bf_hi(xa[k]); }
      xv[8] = bf_lo(xt); xv[9] = bf_hi(xt);
      float sm = 0.f;
#pragma unroll
      for (int k = 0; k < 10; ++k) sm += xv[k];
      const float mean = wave_sum(sm) * (1.0f / DM);
      float ss = 0.f;
#pragma unroll
      for (int k = 0; k < 10; ++k) { const float d = xv[k] - mean; ss = fmaf(d, d, ss); }
      const float var = wave_sum(ss) * (1.0f / DM);
      const float mb = rbf(mean);
      const float sd = rbf(sqrtf(rbf(rbf(var) + 1e-5f)));
      const unsigned n0 = tobf(rbf(bf1(xo0) - mb) / sd);
      const unsigned n1 = tobf(rbf(bf1(xo1) - mb) / sd);
      const unsigned n2 = tobf(rbf(bf1(xo2) - mb) / sd);
      const unsigned d0 = n0 | (n0 << 16), d1 = n1 | (n1 << 16), d2 = n2 | (n2 << 16);
      unsigned* xq = &lds[L_XQ + w * QROWS + (lane & 3) * 40 + (lane >> 2)];
      unsigned* x1 = &lds[L_X1 + w * QROWS + (lane & 7) * 20 + (lane >> 3)];
      xq[0] = d0; xq[16] = d1;
      x1[0] = d0; x1[8] = d1;
      if (lane < 32) { xq[32] = d2; x1[16] = d2; }
    }
    __builtin_amdgcn_fence(__ATOMIC_RELEASE, "wavefront");
    __builtin_amdgcn_wave_barrier();
    __builtin_amdgcn_fence(__ATOMIC_ACQUIRE, "wavefront");
    {
      f32x4 a0 = {0.f, 0.f, 0.f, 0.f}, a1 = {0.f, 0.f, 0.f, 0.f};
#pragma unroll
      for (int i4 = 0; i4 < 10; ++i4) {
        const u32x4 xd = *reinterpret_cast<const u32x4*>(&lds[L_XQ + w * QROWS + r * 40 + i4 * 4]);
        a0 = gemv_mfma(wr[4 * i4 + 0], xd[0], msk8, a0);
        a1 = gemv_mfma(wr[4 * i4 + 1], xd[1], msk8, a1);
        a0 = gemv_mfma(wr[4 * i4 + 2], xd[2], msk8, a0);
        a1 = gemv_mfma(wr[4 * i4 + 3], xd[3], msk8, a1);
      }
      a0 += a1;
      if (c < 8) {
#pragma unroll
        for (int g = 0; g < 4; ++g) ldsf[L_QP + w * DH + 8 * (4 * r + g) + c] = a0[g];
      }
    }
    __builtin_amdgcn_s_setprio(0);
    __syncthreads();
    {
      const unsigned ch = min((unsigned)chunk0 + c8, (unsigned)(FF / 8 - 1));
      const char* p = w1 + (size_t)(QROWS * w) * (FF * 2);
      const unsigned lo = r8 * (FF * 2) + ch * 16;
#pragma unroll
      for (int i = 0; i < 20; ++i) { wr[i] = ld_w(p + (size_t)(8 * i) * (FF * 2) + lo); PACE }
      SB
      const char* p2 = w2 + (size_t)(FS * js) * (DM * 2);
#pragma unroll
      for (int g = 0; g < 4; ++g) {
        const unsigned lo2 = min(16u * w + 4 * g + r, (unsigned)(FS - 1)) * (DM * 2) + c * 16;
#pragma unroll
        for (int s = 0; s < 5; ++s) { wr[20 + 5 * g + s] = ld_w(p2 + s * 256 + lo2); PACE }
      }
      SB
      f32x4 a0 = {0.f, 0.f, 0.f, 0.f}, a1 = {0.f, 0.f, 0.f, 0.f};
#pragma unroll
      for (int i4 = 0; i4 < 5; ++i4) {
        const u32x4 xd = *reinterpret_cast<const u32x4*>(&lds[L_X1 + w * QROWS + r8 * 20 + i4 * 4]);
        a0 = gemv_mfma(wr[4 * i4 + 0], xd[0], msk16, a0);
        a1 = gemv_mfma(wr[4 * i4 + 1], xd[1], msk16, a1);
        a0 = gemv_mfma(wr[4 * i4 + 2], xd[2], msk16, a0);
        a1 = gemv_mfma(wr[4 * i4 + 3], xd[3], msk16, a1);
      }
      a0 += a1;
      if ((r >> 1) == (c >> 3)) {
#pragma unroll
        for (int g = 0; g < 4; ++g) ldsf[L_HP + w * 128 + (r >> 1) * 64 + 8 * (4 * (r & 1) + g) + c8] = a0[g];
      }
      __builtin_amdgcn_fence(__ATOMIC_RELEASE, "workgroup", "local");
      if (lane == 0) atomicAdd(&lds[L_CNT + 1], 1u);
      while (__hip_atomic_load(&lds[L_CNT + 1], __ATOMIC_RELAXED, __HIP_MEMORY_SCOPE_WORKGROUP) < (unsigned)NHLP) __builtin_amdgcn_s_sleep(2);
      __builtin_amdgcn_fence(__ATOMIC_ACQUIRE, "workgroup", "local");
      unsigned hd = 0u;
      {
        const unsigned off = FS * js - chunk0 * 8;
        float sm = 0.f;
#pragma unroll
        for (int ww = 0; ww < 2 * NHLP; ++ww) sm += ldsf[L_HP + ww * 64 + ((off + lane) & 63)];
        const float hb = rbf(rbf(sm) + bf1(b1u));
        const float u2 = 1.5957691216057308f * (hb + 0.044715f * hb * hb * hb);
        const unsigned hv = tobf(hb / (1.0f + __expf(-u2)));
        hd = (lane < (unsigned)FS) ? (hv | (hv << 16)) : 0u;
      }
      unsigned hg[4];
#pragma unroll
      for (int g = 0; g < 4; ++g) hg[g] = __builtin_amdgcn_ds_bpermute((int)((16 * w + 4 * g + r) * 4), (int)hd);
#pragma unroll
      for (int s = 0; s < 5; ++s) {
        f32x4 mm = {0.f, 0.f, 0.f, 0.f};
#pragma unroll
        for (int g = 0; g < 4; ++g) mm = gemv_mfma(wr[20 + 5 * g + s], hg[g], msk8, mm);
        if (c < 8) *reinterpret_cast<f32x4*>(&ldsf[L_PP + ((w * 10 + 5 + s) * 32 + r * 8 + c) * 4]) = mm;
      }
    }
    SB
    {
      const char* wop = wo + (size_t)(h * DH + 32 * w) * (DM * 2);
      const unsigned wlo = r * (DM * 2) + c * 16;
#pragma unroll
      for (int u = 0; u < 40; ++u) { wr[u] = ld_w(wop + (u / 5) * (4 * DM * 2) + (u % 5) * 256 + wlo); PACE }
    }
    SB
    __syncthreads();
    unsigned od[8];
    {
      float mv[NSTR], lv[NSTR], av[NSTR];
#pragma unroll
      for (int ww = 0; ww < NSTR; ++ww) {
        const float* rec = &ldsf[L_MG + ww * MG_REC];
        mv[ww] = rec[0]; lv[ww] = rec[1]; av[ww] = rec[4 + 32 * w + (lane & 31)];
      }
      float M = mv[0];
#pragma unroll
      for (int ww = 1; ww < NSTR; ++ww) M = fmaxf(M, mv[ww]);
      float L = 0.f, o = 0.f;
#pragma unroll
      for (int ww = 0; ww < NSTR; ++ww) {
        const float wt = __builtin_amdgcn_exp2f(mv[ww] - M);
        L = fmaf(wt, lv[ww], L);
        o = fmaf(wt, av[ww], o);
      }
      const unsigned ob = tobf(o / L);
      const unsigned odup = ob | (ob << 16);
#pragma unroll
      for (int g = 0; g < 8; ++g) od[g] = __builtin_amdgcn_ds_bpermute((int)((4 * g + r) * 4), (int)odup);
    }
    {
      f32x4 a[5];
#pragma unroll
      for (int s = 0; s < 5; ++s) a[s] = f32x4{0.f, 0.f, 0.f, 0.f};
#pragma unroll
      for (int g = 0; g < 8; ++g) {
#pragma unroll
        for (int s = 0; s < 5; ++s) a[s] = gemv_mfma(wr[5 * g + s], od[g], msk8, a[s]);
      }
      if (c < 8) {
#pragma unroll
        for (int s = 0; s < 5; ++s) *reinterpret_cast<f32x4*>(&ldsf[L_PP + ((w * 10 + s) * 32 + r * 8 + c) * 4]) = a[s];
      }
    }
    __syncthreads();
  }
  if (tid < 320) {
    f32x4 s = {0.f, 0.f, 0.f, 0.f};
#pragma unroll
    for (int ww = 0; ww < NHLP; ++ww) s += *reinterpret_cast<const f32x4*>(&ldsf[L_PP + (ww * 320 + tid) * 4]);
    *reinterpret_cast<f32x4*>(spart + (size_t)bid * 1280 + tid * 4) = s;
  }
}

__global__ void __launch_bounds__(256) final_kernel(const unsigned short* __restrict__ x, const unsigned short* __restrict__ b2,
                                                    const float* __restrict__ spart, unsigned short* __restrict__ out) {
  __shared__ __attribute__((aligned(16))) float red[2][16][64];
  const int b = blockIdx.x / 10, seg = blockIdx.x % 10;
  const int tid = threadIdx.x, cq = tid & 15, g = tid >> 4;
  const size_t base = (size_t)(b * NH + 2 * g) * 1280 + seg * 64 + cq * 4;
  const f32x4 s0 = *reinterpret_cast<const f32x4*>(spart + base);
  const f32x4 s1 = *reinterpret_cast<const f32x4*>(spart + base + 1280);
  const f32x4 m0 = *reinterpret_cast<const f32x4*>(spart + base + 640);
  const f32x4 m1 = *reinterpret_cast<const f32x4*>(spart + base + 1280 + 640);
  const int p = seg * 64 + (tid & 63);
  const int n = (p >> 7) * 128 + 8 * (4 * ((p >> 5) & 3) + (p & 3)) + ((p >> 2) & 7);
  float b2v = bf1(b2[n]);
  float xv = bf1(x[b * DM + n]);
  asm volatile("" : "+v"(b2v), "+v"(xv));
  *reinterpret_cast<f32x4*>(&red[0][g][cq * 4]) = s0 + s1;
  *reinterpret_cast<f32x4*>(&red[1][g][cq * 4]) = m0 + m1;
  __syncthreads();
  if (tid < 64) {
    float s = 0.f, mm = 0.f;
#pragma unroll
    for (int k = 0; k < 16; ++k) { s += red[0][k][tid]; mm += red[1][k][tid]; }
    const float sb = rbf(s);
    const float mb = rbf(rbf(mm) + b2v);
    const float gt = rbf(mb / (1.0f + __expf(-mb)));
    const float res = xv + rbf(sb * gt);
    out[b * DM + n] = tobf(res);
  }
}

}

extern "C" void kernel_launch(void* const* d_in, const int* in_sizes, int n_in, void* d_out, int out_size, void* d_ws,
                              size_t ws_size, hipStream_t stream) {
  const unsigned short* x = (const unsigned short*)d_in[0];
  const char* kc = (const char*)d_in[1];
  const char* vc = (const char*)d_in[2];
  const char* wq = (const char*)d_in[3];
  const char* wo = (const char*)d_in[4];
  const char* w1 = (const char*)d_in[6];
  const unsigned short* b1 = (const unsigned short*)d_in[7];
  const char* w2 = (const char*)d_in[8];
  const unsigned short* b2 = (const unsigned short*)d_in[9];
  float* spart = (float*)d_ws;
  fused_attn_kernel<<<NB * NH, NT, 0, stream>>>(x, kc, vc, wq, wo, w1, b1, w2, spart);
  final_kernel<<<NB * 10, 256, 0, stream>>>(x, b2, spart, (unsigned short*)d_out);
}
```

```cpp
#define WINN 4
#define WINB 3
#define ALTMASK 3
#include <hip/hip_runtime.h>
#include <hip/hip_bf16.h>
#include <stdint.h>

namespace {

constexpr int NB = 8, DM = 640, NH = 32, SK = 4096, DH = 128, FF = 1728;
constexpr int NW = 8;
constexpr int NT = NW * 64;
constexpr int NHLP = 4, NSTR = 4;
constexpr int FS = FF / NH;
constexpr int QROWS = DM / NHLP;

typedef unsigned u32x4 __attribute__((ext_vector_type(4)));
typedef float f32x4 __attribute__((ext_vector_type(4)));
typedef __bf16 bf16x2 __attribute__((ext_vector_type(2)));
typedef __bf16 bf16x8 __attribute__((ext_vector_type(8)));

__device__ __forceinline__ float bf_lo(unsigned u) { return __builtin_bit_cast(float, u << 16); }
__device__ __forceinline__ float bf_hi(unsigned u) { return __builtin_bit_cast(float, u & 0xffff0000u); }
__device__ __forceinline__ float bf1(unsigned short u) { return __builtin_bit_cast(float, (unsigned)u << 16); }
__device__ __forceinline__ float rbf(float x) { return (float)(__bf16)x; }
__device__ __forceinline__ unsigned short tobf(float x) { return __builtin_bit_cast(unsigned short, (__bf16)x); }
__device__ __forceinline__ unsigned pack2(float lo, float hi) { return (unsigned)tobf(lo) | ((unsigned)tobf(hi) << 16); }

__device__ __forceinline__ float dot2(unsigned k, unsigned q, float acc) {
  return __builtin_amdgcn_fdot2_f32_bf16(__builtin_bit_cast(bf16x2, k), __builtin_bit_cast(bf16x2, q), acc, false);
}
template <int CTRL> __device__ __forceinline__ float dpp(float x) {
  return __builtin_bit_cast(float, __builtin_amdgcn_mov_dpp(__builtin_bit_cast(int, x), CTRL, 0xf, 0xf, true));
}
__device__ __forceinline__ float row16_sum(float x) {
  x += dpp<0xB1>(x);
  x += dpp<0x4E>(x);
  x += dpp<0x141>(x);
  x += dpp<0x140>(x);
  return x;
}
__device__ __forceinline__ float xrow_sum(float x) {
  auto s = __builtin_amdgcn_permlane16_swap(__float_as_uint(x), __float_as_uint(x), false, false);
  x = __uint_as_float(s[0]) + __uint_as_float(s[1]);
  auto t = __builtin_amdgcn_permlane32_swap(__float_as_uint(x), __float_as_uint(x), false, false);
  return __uint_as_float(t[0]) + __uint_as_float(t[1]);
}
__device__ __forceinline__ float xrow_max(float x) {
  auto s = __builtin_amdgcn_permlane16_swap(__float_as_uint(x), __float_as_uint(x), false, false);
  x = fmaxf(__uint_as_float(s[0]), __uint_as_float(s[1]));
  auto t = __builtin_amdgcn_permlane32_swap(__float_as_uint(x), __float_as_uint(x), false, false);
  return fmaxf(__uint_as_float(t[0]), __uint_as_float(t[1]));
}
__device__ __forceinline__ float wave_sum(float x) { return xrow_sum(row16_sum(x)); }

__device__ __forceinline__ u32x4 ld_nt(const char* p) { return __builtin_nontemporal_load(reinterpret_cast<const u32x4*>(p)); }
__device__ __forceinline__ u32x4 ld_w(const char* p) { return __builtin_nontemporal_load(reinterpret_cast<const u32x4*>(p)); }

__device__ __forceinline__ f32x4 gemv_mfma(u32x4 a, unsigned xdup, u32x4 msk, f32x4 c) {
  const u32x4 bsel = {xdup & msk[0], xdup & msk[1], xdup & msk[2], xdup & msk[3]};
  return __builtin_amdgcn_mfma_f32_16x16x32_bf16(__builtin_bit_cast(bf16x8, a), __builtin_bit_cast(bf16x8, bsel), c, 0, 0, 0);
}

constexpr int L_XQ = 0;
constexpr int L_X1 = L_XQ + DM;
constexpr int L_QP = L_X1 + DM;
constexpr int L_HP = L_QP + NHLP * DH;
constexpr int L_MG = L_HP + NHLP * 128;
constexpr int MG_REC = DH + 4;
constexpr int L_PP = L_MG + NSTR * MG_REC;
constexpr int L_SC = L_PP + NHLP * 10 * 128;
constexpr int L_CNT = L_SC + NSTR * 1024;
constexpr int L_END = L_CNT + 4;

__global__ void __launch_bounds__(NT, 2) fused_attn_kernel(
    const unsigned short* __restrict__ x, const char* __restrict__ kc, const char* __restrict__ vc,
    const char* __restrict__ wq, const char* __restrict__ wo, const char* __restrict__ w1,
    const unsigned short* __restrict__ b1, const char* __restrict__ w2, float* __restrict__ spart) {
  __shared__ __attribute__((aligned(16))) unsigned lds[L_END];
  float* ldsf = reinterpret_cast<float*>(lds);
  const int tid = threadIdx.x;
  const unsigned lane = tid & 63;
  const int w = __builtin_amdgcn_readfirstlane(tid >> 6);
  const unsigned r = lane >> 4, c = lane & 15;
  const int bid = blockIdx.x;
  const int h = bid & 31, b = bid >> 5;
#define SB __builtin_amdgcn_sched_barrier(0);
#define PACE SB __builtin_amdgcn_s_sleep(20); SB

  if (w >= NHLP) {
    const int j = w - NHLP;
    const char* kb = kc + (size_t)bid * (SK * DH * 2) + j * 4096;
    const char* vb = vc + (size_t)bid * (SK * DH * 2) + j * 4096;
    const unsigned lo16 = lane * 16;
    u32x4 R[8][4];
#define LOAD_P(d, base) _Pragma("unroll") for (int t = 0; t < 4; ++t) R[d][t] = ld_nt((base) + (d) * 16384 + t * 1024 + lo16);
#define STR2(x) #x
#define STR(x) STR2(x)
#define REFILL1(d, t, base, lim) {                                      \
      if (((t) & ALTMASK) == ALTMASK) asm volatile("s_cmp_eq_u32 %0, 0\n\ts_cbranch_scc1 1f\n\ts_waitcnt vmcnt(" STR(WINB) ")\n1:" : : "s"(lim) : "scc", "memory"); \
      else asm volatile("s_cmp_eq_u32 %0, 0\n\ts_cbranch_scc1 1f\n\ts_waitcnt vmcnt(" STR(WINN) ")\n1:" : : "s"(lim) : "scc", "memory"); \
      R[d][t] = ld_nt((base) + (d) * 16384 + (t) * 1024 + lo16); SB }
#pragma unroll
    for (int d = 0; d < 8; ++d) { LOAD_P(d, kb) SB }
    __syncthreads();
    u32x4 q;
    {
      const float* qp = &ldsf[L_QP + r * DH + c * 8];
      f32x4 s0 = *reinterpret_cast<const f32x4*>(qp);
      f32x4 s1 = *reinterpret_cast<const f32x4*>(qp + 4);
#pragma unroll
      for (int k = 0; k < 4; ++k) { s0[k] = xrow_sum(s0[k]); s1[k] = xrow_sum(s1[k]); }
      q[0] = pack2(s0[0], s0[1]); q[1] = pack2(s0[2], s0[3]); q[2] = pack2(s1[0], s1[1]); q[3] = pack2(s1[2], s1[3]);
    }
    asm volatile("" : "+v"(q));

    float* scw = &ldsf[L_SC + j * 1024];
    const bool c1 = (c & 1) != 0, c2 = (c & 2) != 0;
    float mx = -INFINITY;
    {
      float* sp = scw + r * 4 + (c & 3);
      const char* nb = kb + 8 * 16384;
      for (int it = 0; it < 8; ++it) {
        if (it == 7) nb = vb;
#pragma unroll
        for (int d = 0; d < 8; ++d) {
          float s_[4];
#pragma unroll
          for (int t = 0; t < 4; ++t) {
            float dd = 0.f;
#pragma unroll
            for (int k = 0; k < 4; ++k) dd = dot2(R[d][t][k], q[k], dd);
            asm volatile("" : "+v"(dd));
            s_[t] = dd;
            REFILL1(d, t, nb, 1)
          }
          SB
          const float x0 = (c1 ? s_[1] : s_[0]) + dpp<0xB1>(c1 ? s_[0] : s_[1]);
          const float x1 = (c1 ? s_[3] : s_[2]) + dpp<0xB1>(c1 ? s_[2] : s_[3]);
          float y = (c2 ? x1 : x0) + dpp<0x4E>(c2 ? x0 : x1);
          y += dpp<0x128>(y);
          y += dpp<0x124>(y);
          mx = fmaxf(mx, y);
          if (c < 4) sp[d * 16] = y;
          SB
        }
        sp += 128;
        nb += 8 * 16384;
      }
    }
    constexpr float C2 = 0.08838834764831845f * 1.4426950408889634f;
    float m, l;
    {
      mx = fmaxf(mx, dpp<0xB1>(mx));
      mx = fmaxf(mx, dpp<0x4E>(mx));
      m = xrow_max(mx) * C2;
      __builtin_amdgcn_fence(__ATOMIC_RELEASE, "wavefront");
      __builtin_amdgcn_wave_barrier();
      __builtin_amdgcn_fence(__ATOMIC_ACQUIRE, "wavefront");
      float ls = 0.f;
#pragma unroll
      for (int u = 0; u < 4; ++u) {
        f32x4 v = *reinterpret_cast<const f32x4*>(scw + u * 256 + lane * 4);
#pragma unroll
        for (int k = 0; k < 4; ++k) v[k] = __builtin_amdgcn_exp2f(fmaf(v[k], C2, -m));
        ls += (v[0] + v[1]) + (v[2] + v[3]);
        *reinterpret_cast<f32x4*>(scw + u * 256 + lane * 4) = v;
      }
      l = wave_sum(ls);
      __builtin_amdgcn_fence(__ATOMIC_RELEASE, "wavefront");
      __builtin_amdgcn_wave_barrier();
      __builtin_amdgcn_fence(__ATOMIC_ACQUIRE, "wavefront");
    }
    float acc[8];
#pragma unroll
    for (int k = 0; k < 8; ++k) acc[k] = 0.f;
#define PV_PIECE(d, pp, RF)                                           \
  {                                                                   \
    const f32x4 p_ = *reinterpret_cast<const f32x4*>((pp) + (d) * 16); \
    _Pragma("unroll") for (int t = 0; t < 4; ++t) {                   \
      _Pragma("unroll") for (int k = 0; k < 4; ++k) {                 \
        acc[2 * k] = fmaf(p_[t], bf_lo(R[d][t][k]), acc[2 * k]);      \
        acc[2 * k + 1] = fmaf(p_[t], bf_hi(R[d][t][k]), acc[2 * k + 1]); \
      }                                                               \
      asm volatile("" : "+v"(acc[0]), "+v"(acc[1]), "+v"(acc[2]), "+v"(acc[3]), "+v"(acc[4]), "+v"(acc[5]), "+v"(acc[6]), "+v"(acc[7])); \
      if (RF) REFILL1(d, t, nb, 6 - it)                               \
    }                                                                 \
  }
    {
      const float* pp = scw + r * 4;
      const char* nb = vb + 8 * 16384;
      for (int it = 0; it < 7; ++it) {
#pragma unroll
        for (int d = 0; d < 8; ++d) { PV_PIECE(d, pp, 1) SB }
        pp += 128;
        nb += 8 * 16384;
      }
      const int it = 0;
#pragma unroll
      for (int d = 0; d < 8; ++d) { PV_PIECE(d, pp, 0) SB }
    }
    {
#pragma unroll
      for (int k = 0; k < 8; ++k) acc[k] = xrow_sum(acc[k]);
      float* rec = &ldsf[L_MG + j * MG_REC];
      if (r == 0) {
        *reinterpret_cast<f32x4*>(rec + 4 + c * 8) = f32x4{acc[0], acc[1], acc[2], acc[3]};
        *reinterpret_cast<f32x4*>(rec + 4 + c * 8 + 4) = f32x4{acc[4], acc[5], acc[6], acc[7]};
        if (c == 0) { rec[0] = m; rec[1] = l; }
      }
    }
    __syncthreads();
    __syncthreads();
  } else {
    __builtin_amdgcn_s_setprio(3);
    const int js = (h & 7) * 4 + (h >> 3);
    const unsigned r8 = lane >> 3, c8 = lane & 7;
    const int chunk0 = (FS * js) >> 3;
    const unsigned short* xb = x + b * DM;
    const u32x4 xa = *reinterpret_cast<const u32x4*>(xb + lane * 8);
    const unsigned xt = *reinterpret_cast<const unsigned*>(xb + 512 + lane * 2);
    const unsigned short xo0 = xb[QROWS * w + lane];
    const unsigned short xo1 = xb[QROWS * w + 64 + lane];
    const unsigned short xo2 = xb[QROWS * w + 128 + (lane & 31)];
    const unsigned short b1u = b1[FS * js + min(lane, (unsigned)(FS - 1))];
    SB
    u32x4 wr[40];
    {
      const char* p = wq + (size_t)(QROWS * w) * (NH * DH * 2) + h * (DH * 2);
      const unsigned lo = r * (NH * DH * 2) + c * 16;
#pragma unroll
      for (int i = 0; i < 40; ++i) {
        wr[i] = ld_w(p + (size_t)(4 * i) * (NH * DH * 2) + lo);
        if ((i & 3) == 0) { SB }
      }
    }
    SB
    if (tid == 0) lds[L_CNT + 1] = 0;
    u32x4 msk8, msk16;
#pragma unroll
    for (int k = 0; k < 4; ++k) {
      const unsigned m16 = (c8 == 2u * k) ? 0x0000ffffu : ((c8 == 2u * k + 1) ? 0xffff0000u : 0u);
      msk16[k] = m16;
      msk8[k] = (c < 8) ? m16 : 0u;
    }
    {
      float xv[10];
#pragma unroll
      for (int k = 0; k < 4; ++k) { xv[2 * k] = bf_lo(xa[k]); xv[2 * k + 1] = bf_hi(xa[k]); }
      xv[8] = bf_lo(xt); xv[9] = bf_hi(xt);
      float sm = 0.f;
#pragma unroll
      for (int k = 0; k < 10; ++k) sm += xv[k];
      const float mean = wave_sum(sm) * (1.0f / DM);
      float ss = 0.f;
#pragma unroll
      for (int k = 0; k < 10; ++k) { const float d = xv[k] - mean; ss = fmaf(d, d, ss); }
      const float var = wave_sum(ss) * (1.0f / DM);
      const float mb = rbf(mean);
      const float sd = rbf(sqrtf(rbf(rbf(var) + 1e-5f)));
      const unsigned n0 = tobf(rbf(bf1(xo0) - mb) / sd);
      const unsigned n1 = tobf(rbf(bf1(xo1) - mb) / sd);
      const unsigned n2 = tobf(rbf(bf1(xo2) - mb) / sd);
      const unsigned d0 = n0 | (n0 << 16), d1 = n1 | (n1 << 16), d2 = n2 | (n2 << 16);
      unsigned* xq = &lds[L_XQ + w * QROWS + (lane & 3) * 40 + (lane >> 2)];
      unsigned* x1 = &lds[L_X1 + w * QROWS + (lane & 7) * 20 + (lane >> 3)];
      xq[0] = d0; xq[16] = d1;
      x1[0] = d0; x1[8] = d1;
      if (lane < 32) { xq[32] = d2; x1[16] = d2; }
    }
    __builtin_amdgcn_fence(__ATOMIC_RELEASE, "wavefront");
    __builtin_amdgcn_wave_barrier();
    __builtin_amdgcn_fence(__ATOMIC_ACQUIRE, "wavefront");
    {
      f32x4 a0 = {0.f, 0.f, 0.f, 0.f}, a1 = {0.f, 0.f, 0.f, 0.f};
#pragma unroll
      for (int i4 = 0; i4 < 10; ++i4) {
        const u32x4 xd = *reinterpret_cast<const u32x4*>(&lds[L_XQ + w * QROWS + r * 40 + i4 * 4]);
        a0 = gemv_mfma(wr[4 * i4 + 0], xd[0], msk8, a0);
        a1 = gemv_mfma(wr[4 * i4 + 1], xd[1], msk8, a1);
        a0 = gemv_mfma(wr[4 * i4 + 2], xd[2], msk8, a0);
        a1 = gemv_mfma(wr[4 * i4 + 3], xd[3], msk8, a1);
      }
      a0 += a1;
      if (c < 8) {
#pragma unroll
        for (int g = 0; g < 4; ++g) ldsf[L_QP + w * DH + 8 * (4 * r + g) + c] = a0[g];
      }
    }
    __builtin_amdgcn_s_setprio(0);
    __syncthreads();
    {
      const unsigned ch = min((unsigned)chunk0 + c8, (unsigned)(FF / 8 - 1));
      const char* p = w1 + (size_t)(QROWS * w) * (FF * 2);
      const unsigned lo = r8 * (FF * 2) + ch * 16;
#pragma unroll
      for (int i = 0; i < 20; ++i) { wr[i] = ld_w(p + (size_t)(8 * i) * (FF * 2) + lo); PACE }
      SB
      const char* p2 = w2 + (size_t)(FS * js) * (DM * 2);
#pragma unroll
      for (int g = 0; g < 4; ++g) {
        const unsigned lo2 = min(16u * w + 4 * g + r, (unsigned)(FS - 1)) * (DM * 2) + c * 16;
#pragma unroll
        for (int s = 0; s < 5; ++s) { wr[20 + 5 * g + s] = ld_w(p2 + s * 256 + lo2); PACE }
      }
      SB
      f32x4 a0 = {0.f, 0.f, 0.f, 0.f}, a1 = {0.f, 0.f, 0.f, 0.f};
#pragma unroll
      for (int i4 = 0; i4 < 5; ++i4) {
        const u32x4 xd = *reinterpret_cast<const u32x4*>(&lds[L_X1 + w * QROWS + r8 * 20 + i4 * 4]);
        a0 = gemv_mfma(wr[4 * i4 + 0], xd[0], msk16, a0);
        a1 = gemv_mfma(wr[4 * i4 + 1], xd[1], msk16, a1);
        a0 = gemv_mfma(wr[4 * i4 + 2], xd[2], msk16, a0);
        a1 = gemv_mfma(wr[4 * i4 + 3], xd[3], msk16, a1);
      }
      a0 += a1;
      if ((r >> 1) == (c >> 3)) {
#pragma unroll
        for (int g = 0; g < 4; ++g) ldsf[L_HP + w * 128 + (r >> 1) * 64 + 8 * (4 * (r & 1) + g) + c8] = a0[g];
      }
      __builtin_amdgcn_fence(__ATOMIC_RELEASE, "workgroup", "local");
      if (lane == 0) atomicAdd(&lds[L_CNT + 1], 1u);
      while (__hip_atomic_load(&lds[L_CNT + 1], __ATOMIC_RELAXED, __HIP_MEMORY_SCOPE_WORKGROUP) < (unsigned)NHLP) __builtin_amdgcn_s_sleep(2);
      __builtin_amdgcn_fence(__ATOMIC_ACQUIRE, "workgroup", "local");
      unsigned hd = 0u;
      {
        const unsigned off = FS * js - chunk0 * 8;
        float sm = 0.f;
#pragma unroll
        for (int ww = 0; ww < 2 * NHLP; ++ww) sm += ldsf[L_HP + ww * 64 + ((off + lane) & 63)];
        const float hb = rbf(rbf(sm) + bf1(b1u));
        const float u2 = 1.5957691216057308f * (hb + 0.044715f * hb * hb * hb);
        const unsigned hv = tobf(hb / (1.0f + __expf(-u2)));
        hd = (lane < (unsigned)FS) ? (hv | (hv << 16)) : 0u;
      }
      unsigned hg[4];
#pragma unroll
      for (int g = 0; g < 4; ++g) hg[g] = __builtin_amdgcn_ds_bpermute((int)((16 * w + 4 * g + r) * 4), (int)hd);
#pragma unroll
      for (int s = 0; s < 5; ++s) {
        f32x4 mm = {0.f, 0.f, 0.f, 0.f};
#pragma unroll
        for (int g = 0; g < 4; ++g) mm = gemv_mfma(wr[20 + 5 * g + s], hg[g], msk8, mm);
        if (c < 8) *reinterpret_cast<f32x4*>(&ldsf[L_PP + ((w * 10 + 5 + s) * 32 + r * 8 + c) * 4]) = mm;
      }
    }
    SB
    {
      const char* wop = wo + (size_t)(h * DH + 32 * w) * (DM * 2);
      const unsigned wlo = r * (DM * 2) + c * 16;
#pragma unroll
      for (int u = 0; u < 40; ++u) { wr[u] = ld_w(wop + (u / 5) * (4 * DM * 2) + (u % 5) * 256 + wlo); PACE }
    }
    SB
    __syncthreads();
    unsigned od[8];
    {
      float mv[NSTR], lv[NSTR], av[NSTR];
#pragma unroll
      for (int ww = 0; ww < NSTR; ++ww) {
        const float* rec = &ldsf[L_MG + ww * MG_REC];
        mv[ww] = rec[0]; lv[ww] = rec[1]; av[ww] = rec[4 + 32 * w + (lane & 31)];
      }
      float M = mv[0];
#pragma unroll
      for (int ww = 1; ww < NSTR; ++ww) M = fmaxf(M, mv[ww]);
      float L = 0.f, o = 0.f;
#pragma unroll
      for (int ww = 0; ww < NSTR; ++ww) {
        const float wt = __builtin_amdgcn_exp2f(mv[ww] - M);
        L = fmaf(wt, lv[ww], L);
        o = fmaf(wt, av[ww], o);
      }
      const unsigned ob = tobf(o / L);
      const unsigned odup = ob | (ob << 16);
#pragma unroll
      for (int g = 0; g < 8; ++g) od[g] = __builtin_amdgcn_ds_bpermute((int)((4 * g + r) * 4), (int)odup);
    }
    {
      f32x4 a[5];
#pragma unroll
      for (int s = 0; s < 5; ++s) a[s] = f32x4{0.f, 0.f, 0.f, 0.f};
#pragma unroll
      for (int g = 0; g < 8; ++g) {
#pragma unroll
        for (int s = 0; s < 5; ++s) a[s] = gemv_mfma(wr[5 * g + s], od[g], msk8, a[s]);
      }
      if (c < 8) {
#pragma unroll
        for (int s = 0; s < 5; ++s) *reinterpret_cast<f32x4*>(&ldsf[L_PP + ((w * 10 + s) * 32 + r * 8 + c) * 4]) = a[s];
      }
    }
    __syncthreads();
  }
  if (tid < 320) {
    f32x4 s = {0.f, 0.f, 0.f, 0.f};
#pragma unroll
    for (int ww = 0; ww < NHLP; ++ww) s += *reinterpret_cast<const f32x4*>(&ldsf[L_PP + (ww * 320 + tid) * 4]);
    *reinterpret_cast<f32x4*>(spart + (size_t)bid * 1280 + tid * 4) = s;
  }
}

__global__ void __launch_bounds__(256) final_kernel(const unsigned short* __restrict__ x, const unsigned short* __restrict__ b2,
                                                    const float* __restrict__ spart, unsigned short* __restrict__ out) {
  __shared__ __attribute__((aligned(16))) float red[2][16][64];
  const int b = blockIdx.x / 10, seg = blockIdx.x % 10;
  const int tid = threadIdx.x, cq = tid & 15, g = tid >> 4;
  const size_t base = (size_t)(b * NH + 2 * g) * 1280 + seg * 64 + cq * 4;
  const f32x4 s0 = *reinterpret_cast<const f32x4*>(spart + base);
  const f32x4 s1 = *reinterpret_cast<const f32x4*>(spart + base + 1280);
  const f32x4 m0 = *reinterpret_cast<const f32x4*>(spart + base + 640);
  const f32x4 m1 = *reinterpret_cast<const f32x4*>(spart + base + 1280 + 640);
  const int p = seg * 64 + (tid & 63);
  const int n = (p >> 7) * 128 + 8 * (4 * ((p >> 5) & 3) + (p & 3)) + ((p >> 2) & 7);
  float b2v = bf1(b2[n]);
  float xv = bf1(x[b * DM + n]);
  asm volatile("" : "+v"(b2v), "+v"(xv));
  *reinterpret_cast<f32x4*>(&red[0][g][cq * 4]) = s0 + s1;
  *reinterpret_cast<f32x4*>(&red[1][g][cq * 4]) = m0 + m1;
  __syncthreads();
  if (tid < 64) {
    float s = 0.f, mm = 0.f;
#pragma unroll
    for (int k = 0; k < 16; ++k) { s += red[0][k][tid]; mm += red[1][k][tid]; }
    const float sb = rbf(s);
    const float mb = rbf(rbf(mm) + b2v);
    const float gt = rbf(mb / (1.0f + __expf(-mb)));
    const float res = xv + rbf(sb * gt);
    out[b * DM + n] = tobf(res);
  }
}

}

extern "C" void kernel_launch(void* const* d_in, const int* in_sizes, int n_in, void* d_out, int out_size, void* d_ws,
                              size_t ws_size, hipStream_t stream) {
  const unsigned short* x = (const unsigned short*)d_in[0];
  const char* kc = (const char*)d_in[1];
  const char* vc = (const char*)d_in[2];
  const char* wq = (const char*)d_in[3];
  const char* wo = (const char*)d_in[4];
  const char* w1 = (const char*)d_in[6];
  const unsigned short* b1 = (const unsigned short*)d_in[7];
  const char* w2 = (const char*)d_in[8];
  const unsigned short* b2 = (const unsigned short*)d_in[9];
  float* spart = (float*)d_ws;
  fused_attn_kernel<<<NB * NH, NT, 0, stream>>>(x, kc, vc, wq, wo, w1, b1, w2, spart);
  final_kernel<<<NB * 10, 256, 0, stream>>>(x, b2, spart, (unsigned short*)d_out);
}
```

```cpp
#include <hip/hip_runtime.h>
#include <hip/hip_bf16.h>
#include <stdint.h>

namespace {

constexpr int NB = 8, DM = 640, NH = 32, SK = 4096, DH = 128, FF = 1728;
constexpr int NW = 8;
constexpr int NT = NW * 64;
constexpr int NHLP = 4, NSTR = 4;
constexpr int FS = FF / NH;
constexpr int QROWS = DM / NHLP;

typedef unsigned u32x4 __attribute__((ext_vector_type(4)));
typedef float f32x4 __attribute__((ext_vector_type(4)));
typedef __bf16 bf16x2 __attribute__((ext_vector_type(2)));
typedef __bf16 bf16x8 __attribute__((ext_vector_type(8)));

__device__ __forceinline__ float bf_lo(unsigned u) { return __builtin_bit_cast(float, u << 16); }
__device__ __forceinline__ float bf_hi(unsigned u) { return __builtin_bit_cast(float, u & 0xffff0000u); }
__device__ __forceinline__ float bf1(unsigned short u) { return __builtin_bit_cast(float, (unsigned)u << 16); }
__device__ __forceinline__ float rbf(float x) { return (float)(__bf16)x; }
__device__ __forceinline__ unsigned short tobf(float x) { return __builtin_bit_cast(unsigned short, (__bf16)x); }
__device__ __forceinline__ unsigned pack2(float lo, float hi) { return (unsigned)tobf(lo) | ((unsigned)tobf(hi) << 16); }

__device__ __forceinline__ float dot2(unsigned k, unsigned q, float acc) {
  return __builtin_amdgcn_fdot2_f32_bf16(__builtin_bit_cast(bf16x2, k), __builtin_bit_cast(bf16x2, q), acc, false);
}
template <int CTRL> __device__ __forceinline__ float dpp(float x) {
  return __builtin_bit_cast(float, __builtin_amdgcn_mov_dpp(__builtin_bit_cast(int, x), CTRL, 0xf, 0xf, true));
}
__device__ __forceinline__ float row16_sum(float x) {
  x += dpp<0xB1>(x);
  x += dpp<0x4E>(x);
  x += dpp<0x141>(x);
  x += dpp<0x140>(x);
  return x;
}
__device__ __forceinline__ float xrow_sum(float x) {
  auto s = __builtin_amdgcn_permlane16_swap(__float_as_uint(x), __float_as_uint(x), false, false);
  x = __uint_as_float(s[0]) + __uint_as_float(s[1]);
  auto t = __builtin_amdgcn_permlane32_swap(__float_as_uint(x), __float_as_uint(x), false, false);
  return __uint_as_float(t[0]) + __uint_as_float(t[1]);
}
__device__ __forceinline__ float xrow_max(float x) {
  auto s = __builtin_amdgcn_permlane16_swap(__float_as_uint(x), __float_as_uint(x), false, false);
  x = fmaxf(__uint_as_float(s[0]), __uint_as_float(s[1]));
  auto t = __builtin_amdgcn_permlane32_swap(__float_as_uint(x), __float_as_uint(x), false, false);
  return fmaxf(__uint_as_float(t[0]), __uint_as_float(t[1]));
}
__device__ __forceinline__ float wave_sum(float x) { return xrow_sum(row16_sum(x)); }

__device__ __forceinline__ u32x4 ld_nt(const char* p) { return __builtin_nontemporal_load(reinterpret_cast<const u32x4*>(p)); }
__device__ __forceinline__ u32x4 ld_w(const char* p) { return __builtin_nontemporal_load(reinterpret_cast<const u32x4*>(p)); }

__device__ __forceinline__ f32x4 gemv_mfma(u32x4 a, unsigned xdup, u32x4 msk, f32x4 c) {
  const u32x4 bsel = {xdup & msk[0], xdup & msk[1], xdup & msk[2], xdup & msk[3]};
  return __builtin_amdgcn_mfma_f32_16x16x32_bf16(__builtin_bit_cast(bf16x8, a), __builtin_bit_cast(bf16x8, bsel), c, 0, 0, 0);
}

constexpr int L_XQ = 0;
constexpr int L_X1 = L_XQ + DM;
constexpr int L_QP = L_X1 + DM;
constexpr int L_HP = L_QP + NHLP * DH;
constexpr int L_MG = L_HP + NHLP * 128;
constexpr int MG_REC = DH + 4;
constexpr int L_PP = L_MG + NSTR * MG_REC;
constexpr int L_SC = L_PP + NHLP * 10 * 128;
constexpr int L_CNT = L_SC + NSTR * 1024;
constexpr int L_END = L_CNT + 4;

__global__ void __launch_bounds__(NT, 2) fused_attn_kernel(
    const unsigned short* __restrict__ x, const char* __restrict__ kc, const char* __restrict__ vc,
    const char* __restrict__ wq, const char* __restrict__ wo, const char* __restrict__ w1,
    const unsigned short* __restrict__ b1, const char* __restrict__ w2, float* __restrict__ spart) {
  __shared__ __attribute__((aligned(16))) unsigned lds[L_END];
  float* ldsf = reinterpret_cast<float*>(lds);
  const int tid = threadIdx.x;
  const unsigned lane = tid & 63;
  const int w = __builtin_amdgcn_readfirstlane(tid >> 6);
  const unsigned r = lane >> 4, c = lane & 15;
  const int bid = blockIdx.x;
  const int h = bid & 31, b = bid >> 5;
#define SB __builtin_amdgcn_sched_barrier(0);
#define PACE SB __builtin_amdgcn_s_sleep(20); SB

  if (w >= NHLP) {
    const int j = w - NHLP;
    const char* kb = kc + (size_t)bid * (SK * DH * 2) + j * 4096;
    const char* vb = vc + (size_t)bid * (SK * DH * 2) + j * 4096;
    const unsigned lo16 = lane * 16;
    u32x4 R[8][4];
#define LOAD_P(d, base) _Pragma("unroll") for (int t = 0; t < 4; ++t) R[d][t] = ld_nt((base) + (d) * 16384 + t * 1024 + lo16);
#define REFILL1(d, t, base, lim) {                                      \
      asm volatile("s_cmp_eq_u32 %0, 0\n\ts_cbranch_scc1 1f\n\ts_waitcnt vmcnt(4)\n1:" : : "s"(lim) : "scc", "memory"); \
      R[d][t] = ld_nt((base) + (d) * 16384 + (t) * 1024 + lo16); SB }
#pragma unroll
    for (int d = 0; d < 8; ++d) { LOAD_P(d, kb) SB }
    __syncthreads();
    u32x4 q;
    {
      const float* qp = &ldsf[L_QP + r * DH + c * 8];
      f32x4 s0 = *reinterpret_cast<const f32x4*>(qp);
      f32x4 s1 = *reinterpret_cast<const f32x4*>(qp + 4);
#pragma unroll
      for (int k = 0; k < 4; ++k) { s0[k] = xrow_sum(s0[k]); s1[k] = xrow_sum(s1[k]); }
      q[0] = pack2(s0[0], s0[1]); q[1] = pack2(s0[2], s0[3]); q[2] = pack2(s1[0], s1[1]); q[3] = pack2(s1[2], s1[3]);
    }
    asm volatile("" : "+v"(q));

    float* scw = &ldsf[L_SC + j * 1024];
    const bool c1 = (c & 1) != 0, c2 = (c & 2) != 0;
    float mx = -INFINITY;
    {
      float* sp = scw + r * 4 + (c & 3);
      const char* nb = kb + 8 * 16384;
      for (int it = 0; it < 8; ++it) {
        if (it == 7) nb = vb;
#pragma unroll
        for (int d = 0; d < 8; ++d) {
          float s_[4];
#pragma unroll
          for (int t = 0; t < 4; ++t) {
            float dd = 0.f;
#pragma unroll
            for (int k = 0; k < 4; ++k) dd = dot2(R[d][t][k], q[k], dd);
            asm volatile("" : "+v"(dd));
            s_[t] = dd;
            REFILL1(d, t, nb, 1)
          }
          SB
          const float x0 = (c1 ? s_[1] : s_[0]) + dpp<0xB1>(c1 ? s_[0] : s_[1]);
          const float x1 = (c1 ? s_[3] : s_[2]) + dpp<0xB1>(c1 ? s_[2] : s_[3]);
          float y = (c2 ? x1 : x0) + dpp<0x4E>(c2 ? x0 : x1);
          y += dpp<0x128>(y);
          y += dpp<0x124>(y);
          mx = fmaxf(mx, y);
          if (c < 4) sp[d * 16] = y;
          SB
        }
        sp += 128;
        nb += 8 * 16384;
      }
    }
    constexpr float C2 = 0.08838834764831845f * 1.4426950408889634f;
    float m, l;
    {
      mx = fmaxf(mx, dpp<0xB1>(mx));
      mx = fmaxf(mx, dpp<0x4E>(mx));
      m = xrow_max(mx) * C2;
      __builtin_amdgcn_fence(__ATOMIC_RELEASE, "wavefront");
      __builtin_amdgcn_wave_barrier();
      __builtin_amdgcn_fence(__ATOMIC_ACQUIRE, "wavefront");
      float ls = 0.f;
#pragma unroll
      for (int u = 0; u < 4; ++u) {
        f32x4 v = *reinterpret_cast<const f32x4*>(scw + u * 256 + lane * 4);
#pragma unroll
        for (int k = 0; k < 4; ++k) v[k] = __builtin_amdgcn_exp2f(fmaf(v[k], C2, -m));
        ls += (v[0] + v[1]) + (v[2] + v[3]);
        *reinterpret_cast<f32x4*>(scw + u * 256 + lane * 4) = v;
      }
      l = wave_sum(ls);
      __builtin_amdgcn_fence(__ATOMIC_RELEASE, "wavefront");
      __builtin_amdgcn_wave_barrier();
      __builtin_amdgcn_fence(__ATOMIC_ACQUIRE, "wavefront");
    }
    float acc[8];
#pragma unroll
    for (int k = 0; k < 8; ++k) acc[k] = 0.f;
#define PV_PIECE(d, pp, RF)                                           \
  {                                                                   \
    const f32x4 p_ = *reinterpret_cast<const f32x4*>((pp) + (d) * 16); \
    _Pragma("unroll") for (int t = 0; t < 4; ++t) {                   \
      _Pragma("unroll") for (int k = 0; k < 4; ++k) {                 \
        acc[2 * k] = fmaf(p_[t], bf_lo(R[d][t][k]), acc[2 * k]);      \
        acc[2 * k + 1] = fmaf(p_[t], bf_hi(R[d][t][k]), acc[2 * k + 1]); \
      }                                                               \
      asm volatile("" : "+v"(acc[0]), "+v"(acc[1]), "+v"(acc[2]), "+v"(acc[3]), "+v"(acc[4]), "+v"(acc[5]), "+v"(acc[6]), "+v"(acc[7])); \
      if (RF) REFILL1(d, t, nb, 6 - it)                               \
    }                                                                 \
  }
    {
      const float* pp = scw + r * 4;
      const char* nb = vb + 8 * 16384;
      for (int it = 0; it < 7; ++it) {
#pragma unroll
        for (int d = 0; d < 8; ++d) { PV_PIECE(d, pp, 1) SB }
        pp += 128;
        nb += 8 * 16384;
      }
      const int it = 0;
#pragma unroll
      for (int d = 0; d < 8; ++d) { PV_PIECE(d, pp, 0) SB }
    }
    {
#pragma unroll
      for (int k = 0; k < 8; ++k) acc[k] = xrow_sum(acc[k]);
      float* rec = &ldsf[L_MG + j * MG_REC];
      if (r == 0) {
        *reinterpret_cast<f32x4*>(rec + 4 + c * 8) = f32x4{acc[0], acc[1], acc[2], acc[3]};
        *reinterpret_cast<f32x4*>(rec + 4 + c * 8 + 4) = f32x4{acc[4], acc[5], acc[6], acc[7]};
        if (c == 0) { rec[0] = m; rec[1] = l; }
      }
    }
    __syncthreads();
    __syncthreads();
  } else {
    __builtin_amdgcn_s_setprio(3);
    const int js = (h & 7) * 4 + (h >> 3);
    const unsigned r8 = lane >> 3, c8 = lane & 7;
    const int chunk0 = (FS * js) >> 3;
    const unsigned short* xb = x + b * DM;
    const u32x4 xa = *reinterpret_cast<const u32x4*>(xb + lane * 8);
    const unsigned xt = *reinterpret_cast<const unsigned*>(xb + 512 + lane * 2);
    const unsigned short xo0 = xb[QROWS * w + lane];
    const unsigned short xo1 = xb[QROWS * w + 64 + lane];
    const unsigned short xo2 = xb[QROWS * w + 128 + (lane & 31)];
    const unsigned short b1u = b1[FS * js + min(lane, (unsigned)(FS - 1))];
    SB
    u32x4 wr[40];
    {
      const char* p = wq + (size_t)(QROWS * w) * (NH * DH * 2) + h * (DH * 2);
      const unsigned lo = r * (NH * DH * 2) + c * 16;
#pragma unroll
      for (int i = 0; i < 40; ++i) {
        wr[i] = ld_w(p + (size_t)(4 * i) * (NH * DH * 2) + lo);
        if ((i & 3) == 0) { SB }
      }
    }
    SB
    if (tid == 0) lds[L_CNT + 1] = 0;
    u32x4 msk8, msk16;
#pragma unroll
    for (int k = 0; k < 4; ++k) {
      const unsigned m16 = (c8 == 2u * k) ? 0x0000ffffu : ((c8 == 2u * k + 1) ? 0xffff0000u : 0u);
      msk16[k] = m16;
      msk8[k] = (c < 8) ? m16 : 0u;
    }
    {
      float xv[10];
#pragma unroll
      for (int k = 0; k < 4; ++k) { xv[2 * k] = bf_lo(xa[k]); xv[2 * k + 1] = bf_hi(xa[k]); }
      xv[8] = bf_lo(xt); xv[9] = bf_hi(xt);
      float sm = 0.f;
#pragma unroll
      for (int k = 0; k < 10; ++k) sm += xv[k];
      const float mean = wave_sum(sm) * (1.0f / DM);
      float ss = 0.f;
#pragma unroll
      for (int k = 0; k < 10; ++k) { const float d = xv[k] - mean; ss = fmaf(d, d, ss); }
      const float var = wave_sum(ss) * (1.0f / DM);
      const float mb = rbf(mean);
      const float sd = rbf(sqrtf(rbf(rbf(var) + 1e-5f)));
      const unsigned n0 = tobf(rbf(bf1(xo0) - mb) / sd);
      const unsigned n1 = tobf(rbf(bf1(xo1) - mb) / sd);
      const unsigned n2 = tobf(rbf(bf1(xo2) - mb) / sd);
      const unsigned d0 = n0 | (n0 << 16), d1 = n1 | (n1 << 16), d2 = n2 | (n2 << 16);
      unsigned* xq = &lds[L_XQ + w * QROWS + (lane & 3) * 40 + (lane >> 2)];
      unsigned* x1 = &lds[L_X1 + w * QROWS + (lane & 7) * 20 + (lane >> 3)];
      xq[0] = d0; xq[16] = d1;
      x1[0] = d0; x1[8] = d1;
      if (lane < 32) { xq[32] = d2; x1[16] = d2; }
    }
    __builtin_amdgcn_fence(__ATOMIC_RELEASE, "wavefront");
    __builtin_amdgcn_wave_barrier();
    __builtin_amdgcn_fence(__ATOMIC_ACQUIRE, "wavefront");
    {
      f32x4 a0 = {0.f, 0.f, 0.f, 0.f}, a1 = {0.f, 0.f, 0.f, 0.f};
#pragma unroll
      for (int i4 = 0; i4 < 10; ++i4) {
        const u32x4 xd = *reinterpret_cast<const u32x4*>(&lds[L_XQ + w * QROWS + r * 40 + i4 * 4]);
        a0 = gemv_mfma(wr[4 * i4 + 0], xd[0], msk8, a0);
        a1 = gemv_mfma(wr[4 * i4 + 1], xd[1], msk8, a1);
        a0 = gemv_mfma(wr[4 * i4 + 2], xd[2], msk8, a0);
        a1 = gemv_mfma(wr[4 * i4 + 3], xd[3], msk8, a1);
      }
      a0 += a1;
      if (c < 8) {
#pragma unroll
        for (int g = 0; g < 4; ++g) ldsf[L_QP + w * DH + 8 * (4 * r + g) + c] = a0[g];
      }
    }
    __builtin_amdgcn_s_setprio(0);
    __syncthreads();
    {
      const unsigned ch = min((unsigned)chunk0 + c8, (unsigned)(FF / 8 - 1));
      const char* p = w1 + (size_t)(QROWS * w) * (FF * 2);
      const unsigned lo = r8 * (FF * 2) + ch * 16;
#pragma unroll
      for (int i = 0; i < 20; ++i) { wr[i] = ld_w(p + (size_t)(8 * i) * (FF * 2) + lo); PACE }
      SB
      const char* p2 = w2 + (size_t)(FS * js) * (DM * 2);
#pragma unroll
      for (int g = 0; g < 4; ++g) {
        const unsigned lo2 = min(16u * w + 4 * g + r, (unsigned)(FS - 1)) * (DM * 2) + c * 16;
#pragma unroll
        for (int s = 0; s < 5; ++s) { wr[20 + 5 * g + s] = ld_w(p2 + s * 256 + lo2); PACE }
      }
      SB
      f32x4 a0 = {0.f, 0.f, 0.f, 0.f}, a1 = {0.f, 0.f, 0.f, 0.f};
#pragma unroll
      for (int i4 = 0; i4 < 5; ++i4) {
        const u32x4 xd = *reinterpret_cast<const u32x4*>(&lds[L_X1 + w * QROWS + r8 * 20 + i4 * 4]);
        a0 = gemv_mfma(wr[4 * i4 + 0], xd[0], msk16, a0);
        a1 = gemv_mfma(wr[4 * i4 + 1], xd[1], msk16, a1);
        a0 = gemv_mfma(wr[4 * i4 + 2], xd[2], msk16, a0);
        a1 = gemv_mfma(wr[4 * i4 + 3], xd[3], msk16, a1);
      }
      a0 += a1;
      if ((r >> 1) == (c >> 3)) {
#pragma unroll
        for (int g = 0; g < 4; ++g) ldsf[L_HP + w * 128 + (r >> 1) * 64 + 8 * (4 * (r & 1) + g) + c8] = a0[g];
      }
      __builtin_amdgcn_fence(__ATOMIC_RELEASE, "workgroup", "local");
      if (lane == 0) atomicAdd(&lds[L_CNT + 1], 1u);
      while (__hip_atomic_load(&lds[L_CNT + 1], __ATOMIC_RELAXED, __HIP_MEMORY_SCOPE_WORKGROUP) < (unsigned)NHLP) __builtin_amdgcn_s_sleep(2);
      __builtin_amdgcn_fence(__ATOMIC_ACQUIRE, "workgroup", "local");
      unsigned hd = 0u;
      {
        const unsigned off = FS * js - chunk0 * 8;
        float sm = 0.f;
#pragma unroll
        for (int ww = 0; ww < 2 * NHLP; ++ww) sm += ldsf[L_HP + ww * 64 + ((off + lane) & 63)];
        const float hb = rbf(rbf(sm) + bf1(b1u));
        const float u2 = 1.5957691216057308f * (hb + 0.044715f * hb * hb * hb);
        const unsigned hv = tobf(hb / (1.0f + __expf(-u2)));
        hd = (lane < (unsigned)FS) ? (hv | (hv << 16)) : 0u;
      }
      unsigned hg[4];
#pragma unroll
      for (int g = 0; g < 4; ++g) hg[g] = __builtin_amdgcn_ds_bpermute((int)((16 * w + 4 * g + r) * 4), (int)hd);
#pragma unroll
      for (int s = 0; s < 5; ++s) {
        f32x4 mm = {0.f, 0.f, 0.f, 0.f};
#pragma unroll
        for (int g = 0; g < 4; ++g) mm = gemv_mfma(wr[20 + 5 * g + s], hg[g], msk8, mm);
        if (c < 8) *reinterpret_cast<f32x4*>(&ldsf[L_PP + ((w * 10 + 5 + s) * 32 + r * 8 + c) * 4]) = mm;
      }
    }
    SB
    {
      const char* wop = wo + (size_t)(h * DH + 32 * w) * (DM * 2);
      const unsigned wlo = r * (DM * 2) + c * 16;
#pragma unroll
      for (int u = 0; u < 40; ++u) { wr[u] = ld_w(wop + (u / 5) * (4 * DM * 2) + (u % 5) * 256 + wlo); PACE }
    }
    SB
    __syncthreads();
    unsigned od[8];
    {
      float mv[NSTR], lv[NSTR], av[NSTR];
#pragma unroll
      for (int ww = 0; ww < NSTR; ++ww) {
        const float* rec = &ldsf[L_MG + ww * MG_REC];
        mv[ww] = rec[0]; lv[ww] = rec[1]; av[ww] = rec[4 + 32 * w + (lane & 31)];
      }
      float M = mv[0];
#pragma unroll
      for (int ww = 1; ww < NSTR; ++ww) M = fmaxf(M, mv[ww]);
      float L = 0.f, o = 0.f;
#pragma unroll
      for (int ww = 0; ww < NSTR; ++ww) {
        const float wt = __builtin_amdgcn_exp2f(mv[ww] - M);
        L = fmaf(wt, lv[ww], L);
        o = fmaf(wt, av[ww], o);
      }
      const unsigned ob = tobf(o / L);
      const unsigned odup = ob | (ob << 16);
#pragma unroll
      for (int g = 0; g < 8; ++g) od[g] = __builtin_amdgcn_ds_bpermute((int)((4 * g + r) * 4), (int)odup);
    }
    {
      f32x4 a[5];
#pragma unroll
      for (int s = 0; s < 5; ++s) a[s] = f32x4{0.f, 0.f, 0.f, 0.f};
#pragma unroll
      for (int g = 0; g < 8; ++g) {
#pragma unroll
        for (int s = 0; s < 5; ++s) a[s] = gemv_mfma(wr[5 * g + s], od[g], msk8, a[s]);
      }
      if (c < 8) {
#pragma unroll
        for (int s = 0; s < 5; ++s) *reinterpret_cast<f32x4*>(&ldsf[L_PP + ((w * 10 + s) * 32 + r * 8 + c) * 4]) = a[s];
      }
    }
    __syncthreads();
  }
  if (tid < 320) {
    f32x4 s = {0.f, 0.f, 0.f, 0.f};
#pragma unroll
    for (int ww = 0; ww < NHLP; ++ww) s += *reinterpret_cast<const f32x4*>(&ldsf[L_PP + (ww * 320 + tid) * 4]);
    *reinterpret_cast<f32x4*>(spart + (size_t)bid * 1280 + tid * 4) = s;
  }
}

__global__ void __launch_bounds__(256) final_kernel(const unsigned short* __restrict__ x, const unsigned short* __restrict__ b2,
                                                    const float* __restrict__ spart, unsigned short* __restrict__ out) {
  __shared__ __attribute__((aligned(16))) float red[2][16][64];
  const int b = blockIdx.x / 10, seg = blockIdx.x % 10;
  const int tid = threadIdx.x, cq = tid & 15, g = tid >> 4;
  const size_t base = (size_t)(b * NH + 2 * g) * 1280 + seg * 64 + cq * 4;
  const f32x4 s0 = *reinterpret_cast<const f32x4*>(spart + base);
  const f32x4 s1 = *reinterpret_cast<const f32x4*>(spart + base + 1280);
  const f32x4 m0 = *reinterpret_cast<const f32x4*>(spart + base + 640);
  const f32x4 m1 = *reinterpret_cast<const f32x4*>(spart + base + 1280 + 640);
  const int p = seg * 64 + (tid & 63);
  const int n = (p >> 7) * 128 + 8 * (4 * ((p >> 5) & 3) + (p & 3)) + ((p >> 2) & 7);
  float b2v = bf1(b2[n]);
  float xv = bf1(x[b * DM + n]);
  asm volatile("" : "+v"(b2v), "+v"(xv));
  *reinterpret_cast<f32x4*>(&red[0][g][cq * 4]) = s0 + s1;
  *reinterpret_cast<f32x4*>(&red[1][g][cq * 4]) = m0 + m1;
  __syncthreads();
  if (tid < 64) {
    float s = 0.f, mm = 0.f;
#pragma unroll
    for (int k = 0; k < 16; ++k) { s += red[0][k][tid]; mm += red[1][k][tid]; }
    const float sb = rbf(s);
    const float mb = rbf(rbf(mm) + b2v);
    const float gt = rbf(mb / (1.0f + __expf(-mb)));
    const float res = xv + rbf(sb * gt);
    out[b * DM + n] = tobf(res);
  }
}

}

extern "C" void kernel_launch(void* const* d_in, const int* in_sizes, int n_in, void* d_out, int out_size, void* d_ws,
                              size_t ws_size, hipStream_t stream) {
  const unsigned short* x = (const unsigned short*)d_in[0];
  const char* kc = (const char*)d_in[1];
  const char* vc = (const char*)d_in[2];
  const char* wq = (const char*)d_in[3];
  const char* wo = (const char*)d_in[4];
  const char* w1 = (const char*)d_in[6];
  const unsigned short* b1 = (const unsigned short*)d_in[7];
  const char* w2 = (const char*)d_in[8];
  const unsigned short* b2 = (const unsigned short*)d_in[9];
  float* spart = (float*)d_ws;
  fused_attn_kernel<<<NB * NH, NT, 0, stream>>>(x, kc, vc, wq, wo, w1, b1, w2, spart);
  final_kernel<<<NB * 10, 256, 0, stream>>>(x, b2, spart, (unsigned short*)d_out);
}
```

```cpp
#define SPER 2
#define DP 55
#define STEP 150
#define W0 4
#define W1 5
#define W2 7
#define W3 10
#define T0 350
#include <hip/hip_runtime.h>
#include <hip/hip_bf16.h>
#include <stdint.h>

namespace {

constexpr int NB = 8, DM = 640, NH = 32, SK = 4096, DH = 128, FF = 1728;
constexpr int NW = 8;
constexpr int NT = NW * 64;
constexpr int NHLP = 4, NSTR = 4;
constexpr int FS = FF / NH;
constexpr int QROWS = DM / NHLP;

typedef unsigned u32x4 __attribute__((ext_vector_type(4)));
typedef float f32x4 __attribute__((ext_vector_type(4)));
typedef __bf16 bf16x2 __attribute__((ext_vector_type(2)));
typedef __bf16 bf16x8 __attribute__((ext_vector_type(8)));

__device__ __forceinline__ float bf_lo(unsigned u) { return __builtin_bit_cast(float, u << 16); }
__device__ __forceinline__ float bf_hi(unsigned u) { return __builtin_bit_cast(float, u & 0xffff0000u); }
__device__ __forceinline__ float bf1(unsigned short u) { return __builtin_bit_cast(float, (unsigned)u << 16); }
__device__ __forceinline__ float rbf(float x) { return (float)(__bf16)x; }
__device__ __forceinline__ unsigned short tobf(float x) { return __builtin_bit_cast(unsigned short, (__bf16)x); }
__device__ __forceinline__ unsigned pack2(float lo, float hi) { return (unsigned)tobf(lo) | ((unsigned)tobf(hi) << 16); }

__device__ __forceinline__ float dot2(unsigned k, unsigned q, float acc) {
  return __builtin_amdgcn_fdot2_f32_bf16(__builtin_bit_cast(bf16x2, k), __builtin_bit_cast(bf16x2, q), acc, false);
}
template <int CTRL> __device__ __forceinline__ float dpp(float x) {
  return __builtin_bit_cast(float, __builtin_amdgcn_mov_dpp(__builtin_bit_cast(int, x), CTRL, 0xf, 0xf, true));
}
__device__ __forceinline__ float row16_sum(float x) {
  x += dpp<0xB1>(x);
  x += dpp<0x4E>(x);
  x += dpp<0x141>(x);
  x += dpp<0x140>(x);
  return x;
}
__device__ __forceinline__ float xrow_sum(float x) {
  auto s = __builtin_amdgcn_permlane16_swap(__float_as_uint(x), __float_as_uint(x), false, false);
  x = __uint_as_float(s[0]) + __uint_as_float(s[1]);
  auto t = __builtin_amdgcn_permlane32_swap(__float_as_uint(x), __float_as_uint(x), false, false);
  return __uint_as_float(t[0]) + __uint_as_float(t[1]);
}
__device__ __forceinline__ float xrow_max(float x) {
  auto s = __builtin_amdgcn_permlane16_swap(__float_as_uint(x), __float_as_uint(x), false, false);
  x = fmaxf(__uint_as_float(s[0]), __uint_as_float(s[1]));
  auto t = __builtin_amdgcn_permlane32_swap(__float_as_uint(x), __float_as_uint(x), false, false);
  return fmaxf(__uint_as_float(t[0]), __uint_as_float(t[1]));
}
__device__ __forceinline__ float wave_sum(float x) { return xrow_sum(row16_sum(x)); }

__device__ __forceinline__ u32x4 ld_nt(const char* p) { return __builtin_nontemporal_load(reinterpret_cast<const u32x4*>(p)); }
__device__ __forceinline__ u32x4 ld_w(const char* p) { return __builtin_nontemporal_load(reinterpret_cast<const u32x4*>(p)); }

__device__ __forceinline__ f32x4 gemv_mfma(u32x4 a, unsigned xdup, u32x4 msk, f32x4 c) {
  const u32x4 bsel = {xdup & msk[0], xdup & msk[1], xdup & msk[2], xdup & msk[3]};
  return __builtin_amdgcn_mfma_f32_16x16x32_bf16(__builtin_bit_cast(bf16x8, a), __builtin_bit_cast(bf16x8, bsel), c, 0, 0, 0);
}

constexpr int L_XQ = 0;
constexpr int L_X1 = L_XQ + DM;
constexpr int L_QP = L_X1 + DM;
constexpr int L_HP = L_QP + NHLP * DH;
constexpr int L_MG = L_HP + NHLP * 128;
constexpr int MG_REC = DH + 4;
constexpr int L_PP = L_MG + NSTR * MG_REC;
constexpr int L_SC = L_PP + NHLP * 10 * 128;
constexpr int L_CNT = L_SC + NSTR * 1024;
constexpr int L_END = L_CNT + 4;

__global__ void __launch_bounds__(NT, 2) fused_attn_kernel(
    const unsigned short* __restrict__ x, const char* __restrict__ kc, const char* __restrict__ vc,
    const char* __restrict__ wq, const char* __restrict__ wo, const char* __restrict__ w1,
    const unsigned short* __restrict__ b1, const char* __restrict__ w2, float* __restrict__ spart) {
  __shared__ __attribute__((aligned(16))) unsigned lds[L_END];
  float* ldsf = reinterpret_cast<float*>(lds);
  const int tid = threadIdx.x;
  const unsigned lane = tid & 63;
  const int w = __builtin_amdgcn_readfirstlane(tid >> 6);
  const unsigned r = lane >> 4, c = lane & 15;
  const int bid = blockIdx.x;
  const int h = bid & 31, b = bid >> 5;
#define SB __builtin_amdgcn_sched_barrier(0);
#define PACE SB __builtin_amdgcn_s_sleep(20); SB

  if (w >= NHLP) {
    const int j = w - NHLP;
    const char* kb = kc + (size_t)bid * (SK * DH * 2) + j * 4096;
    const char* vb = vc + (size_t)bid * (SK * DH * 2) + j * 4096;
    const unsigned lo16 = lane * 16;
    u32x4 R[8][4];
#define LOAD_P(d, base) _Pragma("unroll") for (int t = 0; t < 4; ++t) R[d][t] = ld_nt((base) + (d) * 16384 + t * 1024 + lo16);
#define STR2(x) #x
#define STR(x) STR2(x)
    unsigned tsch = (unsigned)__builtin_amdgcn_s_memrealtime() + T0 + 8 * DP;
    int lvl = 0;
#define SCHED(d) if (((d) & (SPER - 1)) == 0) { const int lag_ = (int)((unsigned)__builtin_amdgcn_s_memrealtime() - tsch); \
      lvl = __builtin_amdgcn_readfirstlane((lag_ >= 0 ? 1 : 0) + (lag_ >= STEP ? 1 : 0) + (lag_ >= 2 * STEP ? 1 : 0)); tsch += SPER * DP; }
#define REFILL1(d, t, base, lim) {                                      \
      asm volatile("s_cmp_eq_u32 %0, 0\n\ts_cbranch_scc1 1f\n\ts_waitcnt vmcnt(" STR(W3) ")\n\ts_cmp_ge_i32 %1, 3\n\ts_cbranch_scc1 1f\n\t" \
                   "s_waitcnt vmcnt(" STR(W2) ")\n\ts_cmp_ge_i32 %1, 2\n\ts_cbranch_scc1 1f\n\t"                                        \
                   "s_waitcnt vmcnt(" STR(W1) ")\n\ts_cmp_ge_i32 %1, 1\n\ts_cbranch_scc1 1f\n\t"                                        \
                   "s_waitcnt vmcnt(" STR(W0) ")\n1:" : : "s"(lim), "s"(lvl) : "scc", "memory"); \
      R[d][t] = ld_nt((base) + (d) * 16384 + (t) * 1024 + lo16); SB }
#pragma unroll
    for (int d = 0; d < 8; ++d) { LOAD_P(d, kb) SB }
    __syncthreads();
    u32x4 q;
    {
      const float* qp = &ldsf[L_QP + r * DH + c * 8];
      f32x4 s0 = *reinterpret_cast<const f32x4*>(qp);
      f32x4 s1 = *reinterpret_cast<const f32x4*>(qp + 4);
#pragma unroll
      for (int k = 0; k < 4; ++k) { s0[k] = xrow_sum(s0[k]); s1[k] = xrow_sum(s1[k]); }
      q[0] = pack2(s0[0], s0[1]); q[1] = pack2(s0[2], s0[3]); q[2] = pack2(s1[0], s1[1]); q[3] = pack2(s1[2], s1[3]);
    }
    asm volatile("" : "+v"(q));

    float* scw = &ldsf[L_SC + j * 1024];
    const bool c1 = (c & 1) != 0, c2 = (c & 2) != 0;
    float mx = -INFINITY;
    {
      float* sp = scw + r * 4 + (c & 3);
      const char* nb = kb + 8 * 16384;
      for (int it = 0; it < 8; ++it) {
        if (it == 7) nb = vb;
#pragma unroll
        for (int d = 0; d < 8; ++d) {
          float s_[4];
          SCHED(d)
#pragma unroll
          for (int t = 0; t < 4; ++t) {
            float dd = 0.f;
#pragma unroll
            for (int k = 0; k < 4; ++k) dd = dot2(R[d][t][k], q[k], dd);
            asm volatile("" : "+v"(dd));
            s_[t] = dd;
            REFILL1(d, t, nb, 1)
          }
          SB
          const float x0 = (c1 ? s_[1] : s_[0]) + dpp<0xB1>(c1 ? s_[0] : s_[1]);
          const float x1 = (c1 ? s_[3] : s_[2]) + dpp<0xB1>(c1 ? s_[2] : s_[3]);
          float y = (c2 ? x1 : x0) + dpp<0x4E>(c2 ? x0 : x1);
          y += dpp<0x128>(y);
          y += dpp<0x124>(y);
          mx = fmaxf(mx, y);
          if (c < 4) sp[d * 16] = y;
          SB
        }
        sp += 128;
        nb += 8 * 16384;
      }
    }
    constexpr float C2 = 0.08838834764831845f * 1.4426950408889634f;
    float m, l;
    {
      mx = fmaxf(mx, dpp<0xB1>(mx));
      mx = fmaxf(mx, dpp<0x4E>(mx));
      m = xrow_max(mx) * C2;
      __builtin_amdgcn_fence(__ATOMIC_RELEASE, "wavefront");
      __builtin_amdgcn_wave_barrier();
      __builtin_amdgcn_fence(__ATOMIC_ACQUIRE, "wavefront");
      float ls = 0.f;
#pragma unroll
      for (int u = 0; u < 4; ++u) {
        f32x4 v = *reinterpret_cast<const f32x4*>(scw + u * 256 + lane * 4);
#pragma unroll
        for (int k = 0; k < 4; ++k) v[k] = __builtin_amdgcn_exp2f(fmaf(v[k], C2, -m));
        ls += (v[0] + v[1]) + (v[2] + v[3]);
        *reinterpret_cast<f32x4*>(scw + u * 256 + lane * 4) = v;
      }
      l = wave_sum(ls);
      __builtin_amdgcn_fence(__ATOMIC_RELEASE, "wavefront");
      __builtin_amdgcn_wave_barrier();
      __builtin_amdgcn_fence(__ATOMIC_ACQUIRE, "wavefront");
    }
    float acc[8];
#pragma unroll
    for (int k = 0; k < 8; ++k) acc[k] = 0.f;
#define PV_PIECE(d, pp, RF)                                           \
  {                                                                   \
    const f32x4 p_ = *reinterpret_cast<const f32x4*>((pp) + (d) * 16); \
    _Pragma("unroll") for (int t = 0; t < 4; ++t) {                   \
      _Pragma("unroll") for (int k = 0; k < 4; ++k) {                 \
        acc[2 * k] = fmaf(p_[t], bf_lo(R[d][t][k]), acc[2 * k]);      \
        acc[2 * k + 1] = fmaf(p_[t], bf_hi(R[d][t][k]), acc[2 * k + 1]); \
      }                                                               \
      asm volatile("" : "+v"(acc[0]), "+v"(acc[1]), "+v"(acc[2]), "+v"(acc[3]), "+v"(acc[4]), "+v"(acc[5]), "+v"(acc[6]), "+v"(acc[7])); \
      if (RF) REFILL1(d, t, nb, 6 - it)                               \
    }                                                                 \
  }
    {
      const float* pp = scw + r * 4;
      const char* nb = vb + 8 * 16384;
      for (int it = 0; it < 7; ++it) {
#pragma unroll
        for (int d = 0; d < 8; ++d) { SCHED(d) PV_PIECE(d, pp, 1) SB }
        pp += 128;
        nb += 8 * 16384;
      }
      const int it = 0;
#pragma unroll
      for (int d = 0; d < 8; ++d) { PV_PIECE(d, pp, 0) SB }
    }
    {
#pragma unroll
      for (int k = 0; k < 8; ++k) acc[k] = xrow_sum(acc[k]);
      float* rec = &ldsf[L_MG + j * MG_REC];
      if (r == 0) {
        *reinterpret_cast<f32x4*>(rec + 4 + c * 8) = f32x4{acc[0], acc[1], acc[2], acc[3]};
        *reinterpret_cast<f32x4*>(rec + 4 + c * 8 + 4) = f32x4{acc[4], acc[5], acc[6], acc[7]};
        if (c == 0) { rec[0] = m; rec[1] = l; }
      }
    }
    __syncthreads();
    __syncthreads();
  } else {
    __builtin_amdgcn_s_setprio(3);
    const int js = (h & 7) * 4 + (h >> 3);
    const unsigned r8 = lane >> 3, c8 = lane & 7;
    const int chunk0 = (FS * js) >> 3;
    const unsigned short* xb = x + b * DM;
    const u32x4 xa = *reinterpret_cast<const u32x4*>(xb + lane * 8);
    const unsigned xt = *reinterpret_cast<const unsigned*>(xb + 512 + lane * 2);
    const unsigned short xo0 = xb[QROWS * w + lane];
    const unsigned short xo1 = xb[QROWS * w + 64 + lane];
    const unsigned short xo2 = xb[QROWS * w + 128 + (lane & 31)];
    const unsigned short b1u = b1[FS * js + min(lane, (unsigned)(FS - 1))];
    SB
    u32x4 wr[40];
    {
      const char* p = wq + (size_t)(QROWS * w) * (NH * DH * 2) + h * (DH * 2);
      const unsigned lo = r * (NH * DH * 2) + c * 16;
#pragma unroll
      for (int i = 0; i < 40; ++i) {
        wr[i] = ld_w(p + (size_t)(4 * i) * (NH * DH * 2) + lo);
        if ((i & 3) == 0) { SB }
      }
    }
    SB
    if (tid == 0) lds[L_CNT + 1] = 0;
    u32x4 msk8, msk16;
#pragma unroll
    for (int k = 0; k < 4; ++k) {
      const unsigned m16 = (c8 == 2u * k) ? 0x0000ffffu : ((c8 == 2u * k + 1) ? 0xffff0000u : 0u);
      msk16[k] = m16;
      msk8[k] = (c < 8) ? m16 : 0u;
    }
    {
      float xv[10];
#pragma unroll
      for (int k = 0; k < 4; ++k) { xv[2 * k] = bf_lo(xa[k]); xv[2 * k + 1] = bf_hi(xa[k]); }
      xv[8] = bf_lo(xt); xv[9] = bf_hi(xt);
      float sm = 0.f;
#pragma unroll
      for (int k = 0; k < 10; ++k) sm += xv[k];
      const float mean = wave_sum(sm) * (1.0f / DM);
      float ss = 0.f;
#pragma unroll
      for (int k = 0; k < 10; ++k) { const float d = xv[k] - mean; ss = fmaf(d, d, ss); }
      const float var = wave_sum(ss) * (1.0f / DM);
      const float mb = rbf(mean);
      const float sd = rbf(sqrtf(rbf(rbf(var) + 1e-5f)));
      const unsigned n0 = tobf(rbf(bf1(xo0) - mb) / sd);
      const unsigned n1 = tobf(rbf(bf1(xo1) - mb) / sd);
      const unsigned n2 = tobf(rbf(bf1(xo2) - mb) / sd);
      const unsigned d0 = n0 | (n0 << 16), d1 = n1 | (n1 << 16), d2 = n2 | (n2 << 16);
      unsigned* xq = &lds[L_XQ + w * QROWS + (lane & 3) * 40 + (lane >> 2)];
      unsigned* x1 = &lds[L_X1 + w * QROWS + (lane & 7) * 20 + (lane >> 3)];
      xq[0] = d0; xq[16] = d1;
      x1[0] = d0; x1[8] = d1;
      if (lane < 32) { xq[32] = d2; x1[16] = d2; }
    }
    __builtin_amdgcn_fence(__ATOMIC_RELEASE, "wavefront");
    __builtin_amdgcn_wave_barrier();
    __builtin_amdgcn_fence(__ATOMIC_ACQUIRE, "wavefront");
    {
      f32x4 a0 = {0.f, 0.f, 0.f, 0.f}, a1 = {0.f, 0.f, 0.f, 0.f};
#pragma unroll
      for (int i4 = 0; i4 < 10; ++i4) {
        const u32x4 xd = *reinterpret_cast<const u32x4*>(&lds[L_XQ + w * QROWS + r * 40 + i4 * 4]);
        a0 = gemv_mfma(wr[4 * i4 + 0], xd[0], msk8, a0);
        a1 = gemv_mfma(wr[4 * i4 + 1], xd[1], msk8, a1);
        a0 = gemv_mfma(wr[4 * i4 + 2], xd[2], msk8, a0);
        a1 = gemv_mfma(wr[4 * i4 + 3], xd[3], msk8, a1);
      }
      a0 += a1;
      if (c < 8) {
#pragma unroll
        for (int g = 0; g < 4; ++g) ldsf[L_QP + w * DH + 8 * (4 * r + g) + c] = a0[g];
      }
    }
    __builtin_amdgcn_s_setprio(0);
    __syncthreads();
    {
      const unsigned ch = min((unsigned)chunk0 + c8, (unsigned)(FF / 8 - 1));
      const char* p = w1 + (size_t)(QROWS * w) * (FF * 2);
      const unsigned lo = r8 * (FF * 2) + ch * 16;
#pragma unroll
      for (int i = 0; i < 20; ++i) { wr[i] = ld_w(p + (size_t)(8 * i) * (FF * 2) + lo); PACE }
      SB
      const char* p2 = w2 + (size_t)(FS * js) * (DM * 2);
#pragma unroll
      for (int g = 0; g < 4; ++g) {
        const unsigned lo2 = min(16u * w + 4 * g + r, (unsigned)(FS - 1)) * (DM * 2) + c * 16;
#pragma unroll
        for (int s = 0; s < 5; ++s) { wr[20 + 5 * g + s] = ld_w(p2 + s * 256 + lo2); PACE }
      }
      SB
      f32x4 a0 = {0.f, 0.f, 0.f, 0.f}, a1 = {0.f, 0.f, 0.f, 0.f};
#pragma unroll
      for (int i4 = 0; i4 < 5; ++i4) {
        const u32x4 xd = *reinterpret_cast<const u32x4*>(&lds[L_X1 + w * QROWS + r8 * 20 + i4 * 4]);
        a0 = gemv_mfma(wr[4 * i4 + 0], xd[0], msk16, a0);
        a1 = gemv_mfma(wr[4 * i4 + 1], xd[1], msk16, a1);
        a0 = gemv_mfma(wr[4 * i4 + 2], xd[2], msk16, a0);
        a1 = gemv_mfma(wr[4 * i4 + 3], xd[3], msk16, a1);
      }
      a0 += a1;
      if ((r >> 1) == (c >> 3)) {
#pragma unroll
        for (int g = 0; g < 4; ++g) ldsf[L_HP + w * 128 + (r >> 1) * 64 + 8 * (4 * (r & 1) + g) + c8] = a0[g];
      }
      __builtin_amdgcn_fence(__ATOMIC_RELEASE, "workgroup", "local");
      if (lane == 0) atomicAdd(&lds[L_CNT + 1], 1u);
      while (__hip_atomic_load(&lds[L_CNT + 1], __ATOMIC_RELAXED, __HIP_MEMORY_SCOPE_WORKGROUP) < (unsigned)NHLP) __builtin_amdgcn_s_sleep(2);
      __builtin_amdgcn_fence(__ATOMIC_ACQUIRE, "workgroup", "local");
      unsigned hd = 0u;
      {
        const unsigned off = FS * js - chunk0 * 8;
        float sm = 0.f;
#pragma unroll
        for (int ww = 0; ww < 2 * NHLP; ++ww) sm += ldsf[L_HP + ww * 64 + ((off + lane) & 63)];
        const float hb = rbf(rbf(sm) + bf1(b1u));
        const float u2 = 1.5957691216057308f * (hb + 0.044715f * hb * hb * hb);
        const unsigned hv = tobf(hb / (1.0f + __expf(-u2)));
        hd = (lane < (unsigned)FS) ? (hv | (hv << 16)) : 0u;
      }
      unsigned hg[4];
#pragma unroll
      for (int g = 0; g < 4; ++g) hg[g] = __builtin_amdgcn_ds_bpermute((int)((16 * w + 4 * g + r) * 4), (int)hd);
#pragma unroll
      for (int s = 0; s < 5; ++s) {
        f32x4 mm = {0.f, 0.f, 0.f, 0.f};
#pragma unroll
        for (int g = 0; g < 4; ++g) mm = gemv_mfma(wr[20 + 5 * g + s], hg[g], msk8, mm);
        if (c < 8) *reinterpret_cast<f32x4*>(&ldsf[L_PP + ((w * 10 + 5 + s) * 32 + r * 8 + c) * 4]) = mm;
      }
    }
    SB
    {
      const char* wop = wo + (size_t)(h * DH + 32 * w) * (DM * 2);
      const unsigned wlo = r * (DM * 2) + c * 16;
#pragma unroll
      for (int u = 0; u < 40; ++u) { wr[u] = ld_w(wop + (u / 5) * (4 * DM * 2) + (u % 5) * 256 + wlo); PACE }
    }
    SB
    __syncthreads();
    unsigned od[8];
    {
      float mv[NSTR], lv[NSTR], av[NSTR];
#pragma unroll
      for (int ww = 0; ww < NSTR; ++ww) {
        const float* rec = &ldsf[L_MG + ww * MG_REC];
        mv[ww] = rec[0]; lv[ww] = rec[1]; av[ww] = rec[4 + 32 * w + (lane & 31)];
      }
      float M = mv[0];
#pragma unroll
      for (int ww = 1; ww < NSTR; ++ww) M = fmaxf(M, mv[ww]);
      float L = 0.f, o = 0.f;
#pragma unroll
      for (int ww = 0; ww < NSTR; ++ww) {
        const float wt = __builtin_amdgcn_exp2f(mv[ww] - M);
        L = fmaf(wt, lv[ww], L);
        o = fmaf(wt, av[ww], o);
      }
      const unsigned ob = tobf(o / L);
      const unsigned odup = ob | (ob << 16);
#pragma unroll
      for (int g = 0; g < 8; ++g) od[g] = __builtin_amdgcn_ds_bpermute((int)((4 * g + r) * 4), (int)odup);
    }
    {
      f32x4 a[5];
#pragma unroll
      for (int s = 0; s < 5; ++s) a[s] = f32x4{0.f, 0.f, 0.f, 0.f};
#pragma unroll
      for (int g = 0; g < 8; ++g) {
#pragma unroll
        for (int s = 0; s < 5; ++s) a[s] = gemv_mfma(wr[5 * g + s], od[g], msk8, a[s]);
      }
      if (c < 8) {
#pragma unroll
        for (int s = 0; s < 5; ++s) *reinterpret_cast<f32x4*>(&ldsf[L_PP + ((w * 10 + s) * 32 + r * 8 + c) * 4]) = a[s];
      }
    }
    __syncthreads();
  }
  if (tid < 320) {
    f32x4 s = {0.f, 0.f, 0.f, 0.f};
#pragma unroll
    for (int ww = 0; ww < NHLP; ++ww) s += *reinterpret_cast<const f32x4*>(&ldsf[L_PP + (ww * 320 + tid) * 4]);
    *reinterpret_cast<f32x4*>(spart + (size_t)bid * 1280 + tid * 4) = s;
  }
}

__global__ void __launch_bounds__(256) final_kernel(const unsigned short* __restrict__ x, const unsigned short* __restrict__ b2,
                                                    const float* __restrict__ spart, unsigned short* __restrict__ out) {
  __shared__ __attribute__((aligned(16))) float red[2][16][64];
  const int b = blockIdx.x / 10, seg = blockIdx.x % 10;
  const int tid = threadIdx.x, cq = tid & 15, g = tid >> 4;
  const size_t base = (size_t)(b * NH + 2 * g) * 1280 + seg * 64 + cq * 4;
  const f32x4 s0 = *reinterpret_cast<const f32x4*>(spart + base);
  const f32x4 s1 = *reinterpret_cast<const f32x4*>(spart + base + 1280);
  const f32x4 m0 = *reinterpret_cast<const f32x4*>(spart + base + 640);
  const f32x4 m1 = *reinterpret_cast<const f32x4*>(spart + base + 1280 + 640);
  const int p = seg * 64 + (tid & 63);
  const int n = (p >> 7) * 128 + 8 * (4 * ((p >> 5) & 3) + (p & 3)) + ((p >> 2) & 7);
  float b2v = bf1(b2[n]);
  float xv = bf1(x[b * DM + n]);
  asm volatile("" : "+v"(b2v), "+v"(xv));
  *reinterpret_cast<f32x4*>(&red[0][g][cq * 4]) = s0 + s1;
  *reinterpret_cast<f32x4*>(&red[1][g][cq * 4]) = m0 + m1;
  __syncthreads();
  if (tid < 64) {
    float s = 0.f, mm = 0.f;
#pragma unroll
    for (int k = 0; k < 16; ++k) { s += red[0][k][tid]; mm += red[1][k][tid]; }
    const float sb = rbf(s);
    const float mb = rbf(rbf(mm) + b2v);
    const float gt = rbf(mb / (1.0f + __expf(-mb)));
    const float res = xv + rbf(sb * gt);
    out[b * DM + n] = tobf(res);
  }
}

}

extern "C" void kernel_launch(void* const* d_in, const int* in_sizes, int n_in, void* d_out, int out_size, void* d_ws,
                              size_t ws_size, hipStream_t stream) {
  const unsigned short* x = (const unsigned short*)d_in[0];
  const char* kc = (const char*)d_in[1];
  const char* vc = (const char*)d_in[2];
  const char* wq = (const char*)d_in[3];
  const char* wo = (const char*)d_in[4];
  const char* w1 = (const char*)d_in[6];
  const unsigned short* b1 = (const unsigned short*)d_in[7];
  const char* w2 = (const char*)d_in[8];
  const unsigned short* b2 = (const unsigned short*)d_in[9];
  float* spart = (float*)d_ws;
  fused_attn_kernel<<<NB * NH, NT, 0, stream>>>(x, kc, vc, wq, wo, w1, b1, w2, spart);
  final_kernel<<<NB * 10, 256, 0, stream>>>(x, b2, spart, (unsigned short*)d_out);
}
```

```cpp
#include <hip/hip_runtime.h>
#include <hip/hip_bf16.h>
#include <stdint.h>

namespace {

constexpr int NB = 8, DM = 640, NH = 32, SK = 4096, DH = 128, FF = 1728;
constexpr int NW = 8;
constexpr int NT = NW * 64;
constexpr int NHLP = 4, NSTR = 4;
constexpr int FS = FF / NH;
constexpr int QROWS = DM / NHLP;

typedef unsigned u32x4 __attribute__((ext_vector_type(4)));
typedef float f32x4 __attribute__((ext_vector_type(4)));
typedef __bf16 bf16x2 __attribute__((ext_vector_type(2)));
typedef __bf16 bf16x8 __attribute__((ext_vector_type(8)));

__device__ __forceinline__ float bf_lo(unsigned u) { return __builtin_bit_cast(float, u << 16); }
__device__ __forceinline__ float bf_hi(unsigned u) { return __builtin_bit_cast(float, u & 0xffff0000u); }
__device__ __forceinline__ float bf1(unsigned short u) { return __builtin_bit_cast(float, (unsigned)u << 16); }
__device__ __forceinline__ float rbf(float x) { return (float)(__bf16)x; }
__device__ __forceinline__ unsigned short tobf(float x) { return __builtin_bit_cast(unsigned short, (__bf16)x); }
__device__ __forceinline__ unsigned pack2(float lo, float hi) { return (unsigned)tobf(lo) | ((unsigned)tobf(hi) << 16); }

__device__ __forceinline__ float dot2(unsigned k, unsigned q, float acc) {
  return __builtin_amdgcn_fdot2_f32_bf16(__builtin_bit_cast(bf16x2, k), __builtin_bit_cast(bf16x2, q), acc, false);
}
template <int CTRL> __device__ __forceinline__ float dpp(float x) {
  return __builtin_bit_cast(float, __builtin_amdgcn_mov_dpp(__builtin_bit_cast(int, x), CTRL, 0xf, 0xf, true));
}
__device__ __forceinline__ float row16_sum(float x) {
  x += dpp<0xB1>(x);
  x += dpp<0x4E>(x);
  x += dpp<0x141>(x);
  x += dpp<0x140>(x);
  return x;
}
__device__ __forceinline__ float xrow_sum(float x) {
  auto s = __builtin_amdgcn_permlane16_swap(__float_as_uint(x), __float_as_uint(x), false, false);
  x = __uint_as_float(s[0]) + __uint_as_float(s[1]);
  auto t = __builtin_amdgcn_permlane32_swap(__float_as_uint(x), __float_as_uint(x), false, false);
  return __uint_as_float(t[0]) + __uint_as_float(t[1]);
}
__device__ __forceinline__ float xrow_max(float x) {
  auto s = __builtin_amdgcn_permlane16_swap(__float_as_uint(x), __float_as_uint(x), false, false);
  x = fmaxf(__uint_as_float(s[0]), __uint_as_float(s[1]));
  auto t = __builtin_amdgcn_permlane32_swap(__float_as_uint(x), __float_as_uint(x), false, false);
  return fmaxf(__uint_as_float(t[0]), __uint_as_float(t[1]));
}
__device__ __forceinline__ float wave_sum(float x) { return xrow_sum(row16_sum(x)); }

__device__ __forceinline__ u32x4 ld_nt(const char* p) { return __builtin_nontemporal_load(reinterpret_cast<const u32x4*>(p)); }
__device__ __forceinline__ u32x4 ld_w(const char* p) { return __builtin_nontemporal_load(reinterpret_cast<const u32x4*>(p)); }

__device__ __forceinline__ f32x4 gemv_mfma(u32x4 a, unsigned xdup, u32x4 msk, f32x4 c) {
  const u32x4 bsel = {xdup & msk[0], xdup & msk[1], xdup & msk[2], xdup & msk[3]};
  return __builtin_amdgcn_mfma_f32_16x16x32_bf16(__builtin_bit_cast(bf16x8, a), __builtin_bit_cast(bf16x8, bsel), c, 0, 0, 0);
}

constexpr int L_XQ = 0;
constexpr int L_X1 = L_XQ + DM;
constexpr int L_QP = L_X1 + DM;
constexpr int L_HP = L_QP + NHLP * DH;
constexpr int L_MG = L_HP + NHLP * 128;
constexpr int MG_REC = DH + 4;
constexpr int L_PP = L_MG + NSTR * MG_REC;
constexpr int L_SC = L_PP + NHLP * 10 * 128;
constexpr int L_CNT = L_SC + NSTR * 1024;
constexpr int L_END = L_CNT + 4;

__global__ void __launch_bounds__(NT, 2) fused_attn_kernel(
    const unsigned short* __restrict__ x, const char* __restrict__ kc, const char* __restrict__ vc,
    const char* __restrict__ wq, const char* __restrict__ wo, const char* __restrict__ w1,
    const unsigned short* __restrict__ b1, const char* __restrict__ w2, float* __restrict__ spart) {
  __shared__ __attribute__((aligned(16))) unsigned lds[L_END];
  float* ldsf = reinterpret_cast<float*>(lds);
  const int tid = threadIdx.x;
  const unsigned lane = tid & 63;
  const int w = __builtin_amdgcn_readfirstlane(tid >> 6);
  const unsigned r = lane >> 4, c = lane & 15;
  const int bid = blockIdx.x;
  const int h = bid & 31, b = bid >> 5;
#define SB __builtin_amdgcn_sched_barrier(0);
#define PACE SB __builtin_amdgcn_s_sleep(20); SB

  if (w >= NHLP) {
    const int j = w - NHLP;
    const char* kb = kc + (size_t)bid * (SK * DH * 2) + j * 4096;
    const char* vb = vc + (size_t)bid * (SK * DH * 2) + j * 4096;
    const unsigned lo16 = lane * 16;
    u32x4 R[8][4];
#define LOAD_P(d, base) _Pragma("unroll") for (int t = 0; t < 4; ++t) R[d][t] = ld_nt((base) + (d) * 16384 + t * 1024 + lo16);
    constexpr unsigned SCH_DP = 55, SCH_T0 = 300;
    constexpr int SCH_STEP = 150;
    unsigned tsch = (unsigned)__builtin_amdgcn_s_memrealtime() + SCH_T0 + 8 * SCH_DP;
    int lvl = 0;
#define SCHED(d) if (((d) & 1) == 0) {                                                                              \
      const int lag_ = (int)((unsigned)__builtin_amdgcn_s_memrealtime() - tsch);                                    \
      lvl = __builtin_amdgcn_readfirstlane((lag_ >= 0 ? 1 : 0) + (lag_ >= SCH_STEP ? 1 : 0) + (lag_ >= 2 * SCH_STEP ? 1 : 0)); \
      tsch += 2 * SCH_DP; }
#define REFILL1(d, t, base, lim) {                                                                                  \
      asm volatile("s_cmp_eq_u32 %0, 0\n\ts_cbranch_scc1 1f\n\t"                                                    \
                   "s_waitcnt vmcnt(10)\n\ts_cmp_ge_i32 %1, 3\n\ts_cbranch_scc1 1f\n\t"                              \
                   "s_waitcnt vmcnt(7)\n\ts_cmp_ge_i32 %1, 2\n\ts_cbranch_scc1 1f\n\t"                               \
                   "s_waitcnt vmcnt(5)\n\ts_cmp_ge_i32 %1, 1\n\ts_cbranch_scc1 1f\n\t"                               \
                   "s_waitcnt vmcnt(4)\n1:" : : "s"(lim), "s"(lvl) : "scc", "memory");                               \
      R[d][t] = ld_nt((base) + (d) * 16384 + (t) * 1024 + lo16); SB }
#pragma unroll
    for (int d = 0; d < 8; ++d) { LOAD_P(d, kb) SB }
    __syncthreads();
    u32x4 q;
    {
      const float* qp = &ldsf[L_QP + r * DH + c * 8];
      f32x4 s0 = *reinterpret_cast<const f32x4*>(qp);
      f32x4 s1 = *reinterpret_cast<const f32x4*>(qp + 4);
#pragma unroll
      for (int k = 0; k < 4; ++k) { s0[k] = xrow_sum(s0[k]); s1[k] = xrow_sum(s1[k]); }
      q[0] = pack2(s0[0], s0[1]); q[1] = pack2(s0[2], s0[3]); q[2] = pack2(s1[0], s1[1]); q[3] = pack2(s1[2], s1[3]);
    }
    asm volatile("" : "+v"(q));

    float* scw = &ldsf[L_SC + j * 1024];
    const bool c1 = (c & 1) != 0, c2 = (c & 2) != 0;
    float mx = -INFINITY;
    {
      float* sp = scw + r * 4 + (c & 3);
      const char* nb = kb + 8 * 16384;
      for (int it = 0; it < 8; ++it) {
        if (it == 7) nb = vb;
#pragma unroll
        for (int d = 0; d < 8; ++d) {
          float s_[4];
          SCHED(d)
#pragma unroll
          for (int t = 0; t < 4; ++t) {
            float dd = 0.f;
#pragma unroll
            for (int k = 0; k < 4; ++k) dd = dot2(R[d][t][k], q[k], dd);
            asm volatile("" : "+v"(dd));
            s_[t] = dd;
            REFILL1(d, t, nb, 1)
          }
          SB
          const float x0 = (c1 ? s_[1] : s_[0]) + dpp<0xB1>(c1 ? s_[0] : s_[1]);
          const float x1 = (c1 ? s_[3] : s_[2]) + dpp<0xB1>(c1 ? s_[2] : s_[3]);
          float y = (c2 ? x1 : x0) + dpp<0x4E>(c2 ? x0 : x1);
          y += dpp<0x128>(y);
          y += dpp<0x124>(y);
          mx = fmaxf(mx, y);
          if (c < 4) sp[d * 16] = y;
          SB
        }
        sp += 128;
        nb += 8 * 16384;
      }
    }
    constexpr float C2 = 0.08838834764831845f * 1.4426950408889634f;
    float m, l;
    {
      mx = fmaxf(mx, dpp<0xB1>(mx));
      mx = fmaxf(mx, dpp<0x4E>(mx));
      m = xrow_max(mx) * C2;
      __builtin_amdgcn_fence(__ATOMIC_RELEASE, "wavefront");
      __builtin_amdgcn_wave_barrier();
      __builtin_amdgcn_fence(__ATOMIC_ACQUIRE, "wavefront");
      float ls = 0.f;
#pragma unroll
      for (int u = 0; u < 4; ++u) {
        f32x4 v = *reinterpret_cast<const f32x4*>(scw + u * 256 + lane * 4);
#pragma unroll
        for (int k = 0; k < 4; ++k) v[k] = __builtin_amdgcn_exp2f(fmaf(v[k], C2, -m));
        ls += (v[0] + v[1]) + (v[2] + v[3]);
        *reinterpret_cast<f32x4*>(scw + u * 256 + lane * 4) = v;
      }
      l = wave_sum(ls);
      __builtin_amdgcn_fence(__ATOMIC_RELEASE, "wavefront");
      __builtin_amdgcn_wave_barrier();
      __builtin_amdgcn_fence(__ATOMIC_ACQUIRE, "wavefront");
    }
    float acc[8];
#pragma unroll
    for (int k = 0; k < 8; ++k) acc[k] = 0.f;
#define PV_PIECE(d, pp, RF)                                           \
  {                                                                   \
    const f32x4 p_ = *reinterpret_cast<const f32x4*>((pp) + (d) * 16); \
    _Pragma("unroll") for (int t = 0; t < 4; ++t) {                   \
      _Pragma("unroll") for (int k = 0; k < 4; ++k) {                 \
        acc[2 * k] = fmaf(p_[t], bf_lo(R[d][t][k]), acc[2 * k]);      \
        acc[2 * k + 1] = fmaf(p_[t], bf_hi(R[d][t][k]), acc[2 * k + 1]); \
      }                                                               \
      asm volatile("" : "+v"(acc[0]), "+v"(acc[1]), "+v"(acc[2]), "+v"(acc[3]), "+v"(acc[4]), "+v"(acc[5]), "+v"(acc[6]), "+v"(acc[7])); \
      if (RF) REFILL1(d, t, nb, 6 - it)                               \
    }                                                                 \
  }
    {
      const float* pp = scw + r * 4;
      const char* nb = vb + 8 * 16384;
      for (int it = 0; it < 7; ++it) {
#pragma unroll
        for (int d = 0; d < 8; ++d) { SCHED(d) PV_PIECE(d, pp, 1) SB }
        pp += 128;
        nb += 8 * 16384;
      }
      const int it = 0;
#pragma unroll
      for (int d = 0; d < 8; ++d) { PV_PIECE(d, pp, 0) SB }
    }
    {
#pragma unroll
      for (int k = 0; k < 8; ++k) acc[k] = xrow_sum(acc[k]);
      float* rec = &ldsf[L_MG + j * MG_REC];
      if (r == 0) {
        *reinterpret_cast<f32x4*>(rec + 4 + c * 8) = f32x4{acc[0], acc[1], acc[2], acc[3]};
        *reinterpret_cast<f32x4*>(rec + 4 + c * 8 + 4) = f32x4{acc[4], acc[5], acc[6], acc[7]};
        if (c == 0) { rec[0] = m; rec[1] = l; }
      }
    }
    __syncthreads();
    __syncthreads();
  } else {
    __builtin_amdgcn_s_setprio(3);
    const int js = (h & 7) * 4 + (h >> 3);
    const unsigned r8 = lane >> 3, c8 = lane & 7;
    const int chunk0 = (FS * js) >> 3;
    const unsigned short* xb = x + b * DM;
    const u32x4 xa = *reinterpret_cast<const u32x4*>(xb + lane * 8);
    const unsigned xt = *reinterpret_cast<const unsigned*>(xb + 512 + lane * 2);
    const unsigned short xo0 = xb[QROWS * w + lane];
    const unsigned short xo1 = xb[QROWS * w + 64 + lane];
    const unsigned short xo2 = xb[QROWS * w + 128 + (lane & 31)];
    const unsigned short b1u = b1[FS * js + min(lane, (unsigned)(FS - 1))];
    SB
    u32x4 wr[40];
    {
      const char* p = wq + (size_t)(QROWS * w) * (NH * DH * 2) + h * (DH * 2);
      const unsigned lo = r * (NH * DH * 2) + c * 16;
#pragma unroll
      for (int i = 0; i < 40; ++i) {
        wr[i] = ld_w(p + (size_t)(4 * i) * (NH * DH * 2) + lo);
        if ((i & 3) == 0) { SB }
      }
    }
    SB
    if (tid == 0) lds[L_CNT + 1] = 0;
    u32x4 msk8, msk16;
#pragma unroll
    for (int k = 0; k < 4; ++k) {
      const unsigned m16 = (c8 == 2u * k) ? 0x0000ffffu : ((c8 == 2u * k + 1) ? 0xffff0000u : 0u);
      msk16[k] = m16;
      msk8[k] = (c < 8) ? m16 : 0u;
    }
    {
      float xv[10];
#pragma unroll
      for (int k = 0; k < 4; ++k) { xv[2 * k] = bf_lo(xa[k]); xv[2 * k + 1] = bf_hi(xa[k]); }
      xv[8] = bf_lo(xt); xv[9] = bf_hi(xt);
      float sm = 0.f;
#pragma unroll
      for (int k = 0; k < 10; ++k) sm += xv[k];
      const float mean = wave_sum(sm) * (1.0f / DM);
      float ss = 0.f;
#pragma unroll
      for (int k = 0; k < 10; ++k) { const float d = xv[k] - mean; ss = fmaf(d, d, ss); }
      const float var = wave_sum(ss) * (1.0f / DM);
      const float mb = rbf(mean);
      const float sd = rbf(sqrtf(rbf(rbf(var) + 1e-5f)));
      const unsigned n0 = tobf(rbf(bf1(xo0) - mb) / sd);
      const unsigned n1 = tobf(rbf(bf1(xo1) - mb) / sd);
      const unsigned n2 = tobf(rbf(bf1(xo2) - mb) / sd);
      const unsigned d0 = n0 | (n0 << 16), d1 = n1 | (n1 << 16), d2 = n2 | (n2 << 16);
      unsigned* xq = &lds[L_XQ + w * QROWS + (lane & 3) * 40 + (lane >> 2)];
      unsigned* x1 = &lds[L_X1 + w * QROWS + (lane & 7) * 20 + (lane >> 3)];
      xq[0] = d0; xq[16] = d1;
      x1[0] = d0; x1[8] = d1;
      if (lane < 32) { xq[32] = d2; x1[16] = d2; }
    }
    __builtin_amdgcn_fence(__ATOMIC_RELEASE, "wavefront");
    __builtin_amdgcn_wave_barrier();
    __builtin_amdgcn_fence(__ATOMIC_ACQUIRE, "wavefront");
    {
      f32x4 a0 = {0.f, 0.f, 0.f, 0.f}, a1 = {0.f, 0.f, 0.f, 0.f};
#pragma unroll
      for (int i4 = 0; i4 < 10; ++i4) {
        const u32x4 xd = *reinterpret_cast<const u32x4*>(&lds[L_XQ + w * QROWS + r * 40 + i4 * 4]);
        a0 = gemv_mfma(wr[4 * i4 + 0], xd[0], msk8, a0);
        a1 = gemv_mfma(wr[4 * i4 + 1], xd[1], msk8, a1);
        a0 = gemv_mfma(wr[4 * i4 + 2], xd[2], msk8, a0);
        a1 = gemv_mfma(wr[4 * i4 + 3], xd[3], msk8, a1);
      }
      a0 += a1;
      if (c < 8) {
#pragma unroll
        for (int g = 0; g < 4; ++g) ldsf[L_QP + w * DH + 8 * (4 * r + g) + c] = a0[g];
      }
    }
    __builtin_amdgcn_s_setprio(0);
    __syncthreads();
    {
      const unsigned ch = min((unsigned)chunk0 + c8, (unsigned)(FF / 8 - 1));
      const char* p = w1 + (size_t)(QROWS * w) * (FF * 2);
      const unsigned lo = r8 * (FF * 2) + ch * 16;
#pragma unroll
      for (int i = 0; i < 20; ++i) { wr[i] = ld_w(p + (size_t)(8 * i) * (FF * 2) + lo); PACE }
      SB
      const char* p2 = w2 + (size_t)(FS * js) * (DM * 2);
#pragma unroll
      for (int g = 0; g < 4; ++g) {
        const unsigned lo2 = min(16u * w + 4 * g + r, (unsigned)(FS - 1)) * (DM * 2) + c * 16;
#pragma unroll
        for (int s = 0; s < 5; ++s) { wr[20 + 5 * g + s] = ld_w(p2 + s * 256 + lo2); PACE }
      }
      SB
      f32x4 a0 = {0.f, 0.f, 0.f, 0.f}, a1 = {0.f, 0.f, 0.f, 0.f};
#pragma unroll
      for (int i4 = 0; i4 < 5; ++i4) {
        const u32x4 xd = *reinterpret_cast<const u32x4*>(&lds[L_X1 + w * QROWS + r8 * 20 + i4 * 4]);
        a0 = gemv_mfma(wr[4 * i4 + 0], xd[0], msk16, a0);
        a1 = gemv_mfma(wr[4 * i4 + 1], xd[1], msk16, a1);
        a0 = gemv_mfma(wr[4 * i4 + 2], xd[2], msk16, a0);
        a1 = gemv_mfma(wr[4 * i4 + 3], xd[3], msk16, a1);
      }
      a0 += a1;
      if ((r >> 1) == (c >> 3)) {
#pragma unroll
        for (int g = 0; g < 4; ++g) ldsf[L_HP + w * 128 + (r >> 1) * 64 + 8 * (4 * (r & 1) + g) + c8] = a0[g];
      }
      __builtin_amdgcn_fence(__ATOMIC_RELEASE, "workgroup", "local");
      if (lane == 0) atomicAdd(&lds[L_CNT + 1], 1u);
      while (__hip_atomic_load(&lds[L_CNT + 1], __ATOMIC_RELAXED, __HIP_MEMORY_SCOPE_WORKGROUP) < (unsigned)NHLP) __builtin_amdgcn_s_sleep(2);
      __builtin_amdgcn_fence(__ATOMIC_ACQUIRE, "workgroup", "local");
      unsigned hd = 0u;
      {
        const unsigned off = FS * js - chunk0 * 8;
        float sm = 0.f;
#pragma unroll
        for (int ww = 0; ww < 2 * NHLP; ++ww) sm += ldsf[L_HP + ww * 64 + ((off + lane) & 63)];
        const float hb = rbf(rbf(sm) + bf1(b1u));
        const float u2 = 1.5957691216057308f * (hb + 0.044715f * hb * hb * hb);
        const unsigned hv = tobf(hb / (1.0f + __expf(-u2)));
        hd = (lane < (unsigned)FS) ? (hv | (hv << 16)) : 0u;
      }
      unsigned hg[4];
#pragma unroll
      for (int g = 0; g < 4; ++g) hg[g] = __builtin_amdgcn_ds_bpermute((int)((16 * w + 4 * g + r) * 4), (int)hd);
#pragma unroll
      for (int s = 0; s < 5; ++s) {
        f32x4 mm = {0.f, 0.f, 0.f, 0.f};
#pragma unroll
        for (int g = 0; g < 4; ++g) mm = gemv_mfma(wr[20 + 5 * g + s], hg[g], msk8, mm);
        if (c < 8) *reinterpret_cast<f32x4*>(&ldsf[L_PP + ((w * 10 + 5 + s) * 32 + r * 8 + c) * 4]) = mm;
      }
    }
    SB
    {
      const char* wop = wo + (size_t)(h * DH + 32 * w) * (DM * 2);
      const unsigned wlo = r * (DM * 2) + c * 16;
#pragma unroll
      for (int u = 0; u < 40; ++u) { wr[u] = ld_w(wop + (u / 5) * (4 * DM * 2) + (u % 5) * 256 + wlo); PACE }
    }
    SB
    __syncthreads();
    unsigned od[8];
    {
      float mv[NSTR], lv[NSTR], av[NSTR];
#pragma unroll
      for (int ww = 0; ww < NSTR; ++ww) {
        const float* rec = &ldsf[L_MG + ww * MG_REC];
        mv[ww] = rec[0]; lv[ww] = rec[1]; av[ww] = rec[4 + 32 * w + (lane & 31)];
      }
      float M = mv[0];
#pragma unroll
      for (int ww = 1; ww < NSTR; ++ww) M = fmaxf(M, mv[ww]);
      float L = 0.f, o = 0.f;
#pragma unroll
      for (int ww = 0; ww < NSTR; ++ww) {
        const float wt = __builtin_amdgcn_exp2f(mv[ww] - M);
        L = fmaf(wt, lv[ww], L);
        o = fmaf(wt, av[ww], o);
      }
      const unsigned ob = tobf(o / L);
      const unsigned odup = ob | (ob << 16);
#pragma unroll
      for (int g = 0; g < 8; ++g) od[g] = __builtin_amdgcn_ds_bpermute((int)((4 * g + r) * 4), (int)odup);
    }
    {
      f32x4 a[5];
#pragma unroll
      for (int s = 0; s < 5; ++s) a[s] = f32x4{0.f, 0.f, 0.f, 0.f};
#pragma unroll
      for (int g = 0; g < 8; ++g) {
#pragma unroll
        for (int s = 0; s < 5; ++s) a[s] = gemv_mfma(wr[5 * g + s], od[g], msk8, a[s]);
      }
      if (c < 8) {
#pragma unroll
        for (int s = 0; s < 5; ++s) *reinterpret_cast<f32x4*>(&ldsf[L_PP + ((w * 10 + s) * 32 + r * 8 + c) * 4]) = a[s];
      }
    }
    __syncthreads();
  }
  if (tid < 320) {
    f32x4 s = {0.f, 0.f, 0.f, 0.f};
#pragma unroll
    for (int ww = 0; ww < NHLP; ++ww) s += *reinterpret_cast<const f32x4*>(&ldsf[L_PP + (ww * 320 + tid) * 4]);
    *reinterpret_cast<f32x4*>(spart + (size_t)bid * 1280 + tid * 4) = s;
  }
}

__global__ void __launch_bounds__(256) final_kernel(const unsigned short* __restrict__ x, const unsigned short* __restrict__ b2,
                                                    const float* __restrict__ spart, unsigned short* __restrict__ out) {
  __shared__ __attribute__((aligned(16))) float red[2][16][64];
  const int b = blockIdx.x / 10, seg = blockIdx.x % 10;
  const int tid = threadIdx.x, cq = tid & 15, g = tid >> 4;
  const size_t base = (size_t)(b * NH + 2 * g) * 1280 + seg * 64 + cq * 4;
  const f32x4 s0 = *reinterpret_cast<const f32x4*>(spart + base);
  const f32x4 s1 = *reinterpret_cast<const f32x4*>(spart + base + 1280);
  const f32x4 m0 = *reinterpret_cast<const f32x4*>(spart + base + 640);
  const f32x4 m1 = *reinterpret_cast<const f32x4*>(spart + base + 1280 + 640);
  const int p = seg * 64 + (tid & 63);
  const int n = (p >> 7) * 128 + 8 * (4 * ((p >> 5) & 3) + (p & 3)) + ((p >> 2) & 7);
  float b2v = bf1(b2[n]);
  float xv = bf1(x[b * DM + n]);
  asm volatile("" : "+v"(b2v), "+v"(xv));
  *reinterpret_cast<f32x4*>(&red[0][g][cq * 4]) = s0 + s1;
  *reinterpret_cast<f32x4*>(&red[1][g][cq * 4]) = m0 + m1;
  __syncthreads();
  if (tid < 64) {
    float s = 0.f, mm = 0.f;
#pragma unroll
    for (int k = 0; k < 16; ++k) { s += red[0][k][tid]; mm += red[1][k][tid]; }
    const float sb = rbf(s);
    const float mb = rbf(rbf(mm) + b2v);
    const float gt = rbf(mb / (1.0f + __expf(-mb)));
    const float res = xv + rbf(sb * gt);
    out[b * DM + n] = tobf(res);
  }
}

}

extern "C" void kernel_launch(void* const* d_in, const int* in_sizes, int n_in, void* d_out, int out_size, void* d_ws,
                              size_t ws_size, hipStream_t stream) {
  const unsigned short* x = (const unsigned short*)d_in[0];
  const char* kc = (const char*)d_in[1];
  const char* vc = (const char*)d_in[2];
  const char* wq = (const char*)d_in[3];
  const char* wo = (const char*)d_in[4];
  const char* w1 = (const char*)d_in[6];
  const unsigned short* b1 = (const unsigned short*)d_in[7];
  const char* w2 = (const char*)d_in[8];
  const unsigned short* b2 = (const unsigned short*)d_in[9];
  float* spart = (float*)d_ws;
  fused_attn_kernel<<<NB * NH, NT, 0, stream>>>(x, kc, vc, wq, wo, w1, b1, w2, spart);
  final_kernel<<<NB * 10, 256, 0, stream>>>(x, b2, spart, (unsigned short*)d_out);
}
```
